```python
import torch

def _softmax(x, axis=-1):
    return torch.softmax(x, dim=axis)

def _gelu(x):
    return torch.nn.functional.gelu(x, approximate='tanh')

def _silu(x):
    return torch.nn.functional.silu(x)

def _sigmoid(x):
    return torch.sigmoid(x)

def _layernorm(x, axis=-1, eps=1e-05):
    m = x.mean(dim=axis, keepdim=True)
    v = x.var(dim=axis, keepdim=True, unbiased=False)
    return (x - m) / torch.sqrt(v + eps)

def _scatter_add(tgt, idx, src, axis=0):
    return tgt.clone().index_add_(axis, idx, src)

def _segment_sum(src, seg, n):
    out = torch.zeros((n,) + src.shape[1:], dtype=src.dtype)
    return out.index_add_(0, seg, src)

def _pad(x, pw):
    flat = tuple((v for pair in reversed(pw) for v in pair))
    return torch.nn.functional.pad(x, flat)
kernel_name = 'standalone__autoregressive_conv_wavernn__speech'

def setup_inputs():

    _w = lambda a: (lambda t: t.to(torch.bfloat16) if t.is_floating_point() else t)(torch.as_tensor(a))
    B, T, D, K = (2, 16000, 256, 15)
    return {'x': _w((lambda *s: torch.randn(*s, dtype=torch.bfloat16) * 0.1)(B, T, D)), 'w': _w((lambda *s: torch.randn(*s, dtype=torch.bfloat16) * 0.1)(K, D, 2 * D))}

def reference(x, w):
    B, T, D = x.shape
    K = w.shape[0]
    dil = 2
    pad = (K - 1) * dil
    xp = _pad(x, ((0, 0), (pad, 0), (0, 0)))
    h = torch.einsum('btd,de->bte', xp[:, 0:T, :], w[0])
    for i in range(1, K):
        h = h + torch.einsum('btd,de->bte', xp[:, i * dil:i * dil + T, :], w[i])
    f, g = (h[..., :D].clone(), h[..., D:].clone())
    return torch.tanh(f) * _sigmoid(g)
```

```cpp
#include <hip/hip_runtime.h>
#include <hip/hip_bf16.h>
typedef __attribute__((ext_vector_type(8))) __bf16 bf16x8;
typedef __attribute__((ext_vector_type(4))) float f32x4;
typedef unsigned short u16;
constexpr int B_=2, T_=16000, D_=256, K_=15;

__global__ void wt_kernel(const u16* __restrict__ w, u16* __restrict__ wt){
  int idx = blockIdx.x*256+threadIdx.x;
  int d = idx & 255, e = (idx>>8)&511, i = idx>>17;
  wt[idx] = w[(i*256+d)*512+e];
}
__global__ __launch_bounds__(256) void conv_kernel(const u16* __restrict__ x, const u16* __restrict__ wt, __hip_bfloat16* __restrict__ out){
  int wid = threadIdx.x>>6, lane = threadIdx.x&63;
  int tile = blockIdx.x;
  int b = tile / 250, t0 = (tile % 250)*64;
  int ch0 = (blockIdx.y*4+wid)*32;
  int r = lane&15, q = lane>>4;
  f32x4 acc[4][4] = {};
  for (int i=0;i<K_;i++){
    for (int ks=0;ks<8;ks++){
      bf16x8 a[4], bb[4];
      for (int n=0;n<4;n++){ int e = (n>>1)*256 + ch0 + (n&1)*16 + r;
        a[n] = *(const bf16x8*)(wt + ((size_t)(i*512+e)*256 + ks*32 + q*8)); }
      for (int m=0;m<4;m++){ int t = t0 + m*16 + r + 2*i - 28;
        bf16x8 v = {}; if (t>=0) v = *(const bf16x8*)(x + ((size_t)(b*T_+t)*256 + ks*32+q*8)); bb[m]=v; }
      for (int m=0;m<4;m++) for (int n=0;n<4;n++)
        acc[m][n] = __builtin_amdgcn_mfma_f32_16x16x32_bf16(a[n], bb[m], acc[m][n],0,0,0);
    }
  }
  for (int m=0;m<4;m++) for (int n=0;n<2;n++) for (int j=0;j<4;j++){
    float f = acc[m][n][j], g = acc[m][n+2][j];
    float o = tanhf(f) * (1.f/(1.f+__expf(-g)));
    int t = t0+m*16+r; int c = ch0+n*16+q*4+j;
    out[(size_t)(b*T_+t)*256 + c] = __float2bfloat16(o);
  }
}
extern "C" void kernel_launch(void* const* d_in, const int* in_sizes, int n_in,
                              void* d_out, int out_size, void* d_ws, size_t ws_size,
                              hipStream_t stream) {
  const u16* x = (const u16*)d_in[0]; const u16* w = (const u16*)d_in[1];
  u16* wt = (u16*)d_ws;
  wt_kernel<<<K_*512, 256, 0, stream>>>(w, wt);
  conv_kernel<<<dim3(500,2), 256, 0, stream>>>(x, wt, (__hip_bfloat16*)d_out);
}
```

```cpp
#include <hip/hip_runtime.h>

constexpr int LDS_BYTES = 158976;

__global__ __launch_bounds__(256, 1) void conv_kernel(const char* x, const char* wraw, char* ws, char* out) {
  __shared__ __attribute__((aligned(16))) char lds[LDS_BYTES];
  asm volatile(
".macro M d,w,x\n"
"v_mfma_f32_16x16x32_bf16 a[\\d:\\d+3], a[\\w:\\w+3], a[\\x:\\x+3], a[\\d:\\d+3]\n"
".endm\n"
".macro MZ d,w,x\n"
"v_mfma_f32_16x16x32_bf16 a[\\d:\\d+3], a[\\w:\\w+3], a[\\x:\\x+3], 0\n"
".endm\n"
".macro MC d,w,x,c\n"
"v_mfma_f32_16x16x32_bf16 a[\\d:\\d+3], a[\\w:\\w+3], a[\\x:\\x+3], a[\\c:\\c+3]\n"
".endm\n"
".macro N d,w,x\n"
"v_mfma_f32_16x16x32_bf16 v[\\d:\\d+3], a[\\w:\\w+3], a[\\x:\\x+3], v[\\d:\\d+3]\n"
".endm\n"
".macro NZ d,w,x\n"
"v_mfma_f32_16x16x32_bf16 v[\\d:\\d+3], a[\\w:\\w+3], a[\\x:\\x+3], 0\n"
".endm\n"
".macro D d,b,o\n"
"ds_read_b128 a[\\d:\\d+3], v\\b offset:\\o\n"
".endm\n"
".macro K d,o\n"
"global_load_dwordx4 a[\\d:\\d+3], v15, s[44:45] offset:\\o sc1\n"
".endm\n"
".macro G d,b,o\n"
"buffer_load_dwordx4 v[\\d:\\d+3], v\\b, s[28:31], 0 offen offset:\\o \n"
".endm\n"
".macro H d,b,o\n"
"buffer_load_dword v\\d, v\\b, s[28:31], 0 offen offset:\\o \n"
".endm\n"
".macro S b,d,o\n"
"ds_write_b128 v\\b, v[\\d:\\d+3] offset:\\o\n"
".endm\n"
".macro S2 b,d,o\n"
"ds_write_b64 v\\b, v[\\d:\\d+1] offset:\\o\n"
".endm\n"
".macro U b,d,o\n"
"ds_write_b32 v\\b, v\\d offset:\\o\n"
".endm\n"
".macro L d,a\n"
"v_lshlrev_b32 v\\d, 16, v\\a\n"
".endm\n"
".macro A d,a\n"
"v_and_b32 v\\d, s33, v\\a\n"
".endm\n"
".macro X d,a,b\n"
"v_add_f32 v\\d, v\\a, v\\b\n"
".endm\n"
".macro Y d,a,b\n"
"v_sub_f32 v\\d, v\\a, v\\b\n"
".endm\n"
".macro Z d,a,b\n"
"v_mul_f32 v\\d, v\\a, v\\b\n"
".endm\n"
".macro C d,a,b\n"
"v_cvt_pk_bf16_f32 v\\d, v\\a, v\\b\n"
".endm\n"
".macro RA d,a\n"
"v_accvgpr_read_b32 v\\d, a\\a\n"
".endm\n"
".macro WA d,a\n"
"v_accvgpr_write_b32 a\\d, v\\a\n"
".endm\n"
".macro E1 d,a\n"
"v_mul_f32 v\\d, s46, v\\a\n"
".endm\n"
".macro E2 d,a\n"
"v_mul_f32 v\\d, s34, v\\a\n"
".endm\n"
".macro EX d,a\n"
"v_exp_f32 v\\d, v\\a\n"
".endm\n"
".macro EA d,a\n"
"v_exp_f32 v\\d, -abs(v\\a)\n"
".endm\n"
".macro RC d,a\n"
"v_rcp_f32 v\\d, v\\a\n"
".endm\n"
".macro E3 d,a\n"
"v_fma_f32 v\\d, v\\a, s20, s20\n"
".endm\n"
".macro E4 d,a,b\n"
"v_fma_f32 v\\d, v\\a, v\\b, v\\b\n"
".endm\n"
".macro E5 d,a\n"
"v_sub_f32 v\\d, s22, v\\a\n"
".endm\n"
".macro E6 d,a,b\n"
"v_med3_f32 v\\d, v\\a, -v\\b, v\\b\n"
".endm\n"
".macro Q1 d,a\n"
"v_pk_mul_f32 v[\\d:\\d+1], v[\\a:\\a+1], s[46:47] op_sel_hi:[1,0]\n"
".endm\n"
".macro Q2 d,a\n"
"v_pk_mul_f32 v[\\d:\\d+1], v[\\a:\\a+1], s[34:35] op_sel_hi:[1,0]\n"
".endm\n"
".macro Q3 d,a\n"
"v_pk_fma_f32 v[\\d:\\d+1], v[\\a:\\a+1], s[20:21], s[20:21] op_sel_hi:[1,0,0]\n"
".endm\n"
".macro Q4 d,a,b\n"
"v_pk_fma_f32 v[\\d:\\d+1], v[\\a:\\a+1], v[\\b:\\b+1], v[\\b:\\b+1]\n"
".endm\n"
".macro Q5 d,a\n"
"v_pk_add_f32 v[\\d:\\d+1], s[22:23], v[\\a:\\a+1] op_sel_hi:[0,1] neg_lo:[0,1] neg_hi:[0,1]\n"
".endm\n"
".macro QZ d,a,b\n"
"v_pk_mul_f32 v[\\d:\\d+1], v[\\a:\\a+1], v[\\b:\\b+1]\n"
".endm\n"
".macro T d,o\n"
"ds_write_b64 v8, v[\\d:\\d+1] offset:\\o\n"
".endm\n"
".macro P d,s,o\n"
"buffer_store_dwordx4 v[\\d:\\d+3], v16, s[36:39], s\\s offen offset:\\o sc1\n"
".endm\n"
"s_load_dwordx2 s[24:25], %0, 8\ns_load_dwordx2 s[14:15], %0, 16\ns_load_dwordx2 s[28:29], %0, 0\ns_load_dwordx2 s[36:37], %0, 24\nv_mbcnt_lo_u32_b32 v120, -1, 0\n"
"v_mbcnt_hi_u32_b32 v120, -1, v120\nv_add_u32 v121, %2, v120\ns_getpc_b64 s[20:21]\ns_bfe_u32 s56, %1, 0x40003\ns_lshl_b32 s56, s56, 13\nv_lshl_add_u32 v255, v121, 5, s56\n"
"v_min_u32 v255, .Lconv_code_end - . , v255\nglobal_load_dword v255, v255, s[20:21]\ns_and_b32 s17, %1, 7\ns_lshr_b32 s56, %1, 5\ns_lshl3_add_u32 s17, s17, s56\n"
"s_bfe_u32 s18, %1, 0x20003\ns_lshr_b32 s56, s17, 4\ns_and_b32 s57, s18, 2\ns_lshl1_add_u32 s56, s57, s56\ns_lshl_b32 s56, s56, 1\ns_lshr_b32 s56, 55341, s56\ns_and_b32 s56, s56, 3\n"
"s_lshl4_add_u32 s56, s56, 15\ns_andn2_b32 s57, 15, s17\ns_cselect_b32 s17, s17, s56\ns_lshr_b32 s16, %2, 6\ns_lshr_b32 s56, s17, 3\ns_lshl_b32 s56, s56, 8\ns_and_b32 s57, s17, 4\n"
"s_add_u32 s56, s56, s57\ns_add_u32 s56, s56, s18\ns_lshl_b32 s56, s56, 7\nv_lshrrev_b32 v122, 3, v121\nv_and_b32 v196, 7, v121\nv_lshl_add_u32 v123, v196, 4, s56\n"
"v_lshl_add_u32 v253, v122, 10, v123\ns_and_b32 s59, s17, 3\ns_lshl_b32 s59, s59, 20\ns_sub_u32 s58, s59, 0x40000\ns_cselect_b32 s58, 0x80000000, s58\nv_add_u32 v252, s58, v253\n"
"s_add_u32 s60, s59, 0x40000\ns_add_u32 s61, s59, 0x80000\ns_mov_b32 s26, 0x3c0000\ns_mov_b32 s27, 0x20000\ns_lshl_b32 s56, s18, 11\ns_add_u32 s56, s56, 9437184\ns_mov_b32 s42, 2048\n"
"s_mov_b32 s43, 0x20000\nv_lshlrev_b32 v127, 5, v120\ns_waitcnt lgkmcnt(0)\ns_and_b32 s25, s25, 0xffff\nbuffer_load_dwordx4 v[200:203], v252, s[24:27], 0 offen\n"
"buffer_load_dwordx4 v[204:207], v253, s[24:27], s59 offen\nbuffer_load_dwordx4 v[208:211], v253, s[24:27], s60 offen\nbuffer_load_dwordx4 v[212:215], v253, s[24:27], s61 offen\n"
"s_add_u32 s40, s14, s56\ns_addc_u32 s41, s15, 0\ns_and_b32 s41, s41, 0xffff\nbuffer_load_dwordx3 v[124:126], v127, s[40:43], 0 offen sc1\ns_and_b32 s19, s17, 31\n"
"s_lshl_b32 s19, s19, 9\ns_lshr_b32 s47, s17, 5\ns_cmp_eq_u32 s17, 31\ns_cselect_b32 s57, s16, 0\ns_bitcmp1_b32 s57, 1\ns_cselect_b32 s47, 1, s47\ns_cselect_b32 s19, 15616, s19\n"
"s_mul_i32 s47, s47, 8192000\ns_mov_b32 s30, 8192000\ns_mov_b32 s31, 0x20000\ns_sub_u32 s57, s19, 32\nv_and_b32 v122, 0xf0, v121\nv_bfe_u32 v123, v121, 3, 1\n"
"v_lshl_add_u32 v122, v122, 1, v123\nv_add_lshl_u32 v122, v122, s57, 9\nv_lshl_add_u32 v8, v196, 4, v122\nv_add_u32 v9, 0x1000, v8\nv_add_u32 v10, 0x2000, v8\n"
"v_add_u32 v11, 0x3000, v8\nv_add_u32 v12, 0x4000, v8\ns_lshl3_add_u32 s57, s16, s19\ns_add_u32 s57, s57, 482\nv_lshrrev_b32 v123, 5, v120\nv_add_lshl_u32 v122, v123, s57, 9\n"
"v_and_b32 v196, 31, v120\nv_lshl_add_u32 v13, v196, 2, v122\nv_add_u32 v14, 0x1000, v13\nv_and_b32 v122, 15, v120\nv_and_b32 v123, 5, v120\nv_lshl_add_u32 v123, v123, 2, 8\n"
"v_cmp_gt_u32 vcc, 8, v122\ns_nop 1\nv_cndmask_b32 v122, v123, v122, vcc\nv_lshrrev_b32 v123, 4, v121\nv_lshl_add_u32 v122, v123, 5, v122\ns_lshl_b32 s57, s18, 7\n"
"s_add_u32 s57, s57, s19\ns_sub_u32 s57, s57, 24\ns_lshl_b32 s57, s57, 9\nv_lshl_add_u32 v17, v122, 7, s57\nv_and_b32 v122, 14, v120\nv_lshlrev_b32 v122, 1, v122\n"
"v_max_u32 v122, 8, v122\nv_lshrrev_b32 v123, 4, v121\nv_lshl_add_u32 v122, v123, 5, v122\ns_lshl_b32 s57, s18, 7\ns_add_u32 s57, s57, s19\ns_sub_u32 s57, s57, 24\n"
"s_lshl_b32 s57, s57, 9\nv_lshl_add_u32 v199, v122, 7, s57\ns_add_u32 s28, s28, s47\ns_addc_u32 s29, s29, 0\ns_and_b32 s29, s29, 0xffff\nG 128,9,0\nG 132,10,0\nG 136,11,0\n"
"G 140,12,0\nH 20,13,3072\ns_mul_i32 s56, s18, 2359296\ns_add_u32 s44, s14, s56\ns_addc_u32 s45, s15, 0\ns_lshl2_add_u32 s57, s17, s16\ns_lshl_b32 s57, s57, 8\n"
"v_and_b32 v122, 15, v120\nv_lshl_add_u32 v122, v122, 4, s57\nv_lshrrev_b32 v196, 4, v120\ns_mov_b32 s56, 589824\nv_mad_u32_u24 v198, v196, s56, v122\ns_lshl_b32 s57, s16, 12\n"
"s_add_u32 s57, s57, %3\ns_add_u32 s57, s57, 70656\nv_lshl_add_u32 v254, v120, 4, s57\nv_and_b32 v122, 12, v120\nv_add_u32 v122, v122, v196\nv_and_b32 v123, 3, v120\n"
"v_lshl_add_u32 v122, v122, 2, v123\nv_lshl_add_u32 v252, v122, 3, s57\ns_mov_b32 s33, 0xffff0000\ns_mov_b32 s34, 0x4038aa3b\ns_bitcmp1_b32 s17, 2\n"
"s_cselect_b32 s34, 0xbfb8aa3b, s34\ns_and_b32 s19, s17, 31\ns_lshl_b32 s19, s19, 9\ns_lshr_b32 s47, s17, 5\ns_cmp_eq_u32 s17, 31\ns_cselect_b32 s57, s16, 0\ns_bitcmp1_b32 s57, 0\n"
"s_cselect_b32 s47, 1, s47\ns_cselect_b32 s19, 15616, s19\ns_mul_i32 s47, s47, 8192000\ns_add_u32 s36, s36, s47\ns_addc_u32 s37, s37, 0\ns_and_b32 s37, s37, 0xffff\n"
"s_mov_b32 s38, 8192000\ns_mov_b32 s39, 0x20000\ns_mov_b32 s20, 0x3fb8aa3b\ns_mov_b32 s22, 0x4038aa3d\ns_mov_b32 s48, 0\ns_mov_b32 s50, 8192\ns_mov_b32 s52, 16384\n"
"s_mov_b32 s54, 24576\ns_mul_i32 s56, s16, 589824\ns_add_u32 s56, s56, 0x1000\nv_lshl_add_u32 v15, v120, 4, s56\ns_mul_i32 s57, s16, 256\ns_add_u32 s57, s57, %3\n"
"s_add_u32 s57, s57, 16640\nv_lshl_add_u32 v6, v120, 2, s57\nv_add_u32 v7, 70656, v6\nv_bfe_u32 v122, v121, 4, 1\nv_mul_u32_u24 v122, 1024, v122\nv_lshrrev_b32 v123, 5, v121\n"
"v_mul_u32_u24 v123, 2080, v123\nv_and_b32 v196, 15, v121\nv_lshl_add_u32 v196, v196, 4, %3\nv_add3_u32 v3, v122, v123, v196\nv_add_u32 v4, 52992, v3\nv_add_u32 v5, 105984, v3\n"
"v_bfe_u32 v122, v120, 1, 3\nv_and_b32 v123, 1, v120\nv_lshrrev_b32 v196, 4, v120\nv_mul_u32_u24 v0, 2080, v122\nv_lshl_add_u32 v0, v123, 7, v0\nv_lshl_add_u32 v0, v196, 4, v0\n"
"v_add_u32 v0, %3, v0\nv_add_u32 v1, 52992, v0\nv_add_u32 v2, 105984, v0\ns_and_b32 s57, s16, 1\ns_lshl_b32 s57, s57, 8\ns_lshr_b32 s58, s16, 1\ns_lshl_b32 s58, s58, 3\n"
"s_add_u32 s57, s57, s58\ns_add_u32 s57, s57, s19\nv_bfe_u32 v123, v120, 3, 1\nv_lshl_add_u32 v122, v196, 6, v123\nv_add_lshl_u32 v122, v122, s57, 9\nv_and_b32 v123, 7, v120\n"
"s_lshl_b32 s57, s18, 7\nv_lshl_add_u32 v123, v123, 4, s57\nv_add_u32 v16, v122, v123\ns_add_u32 s57, %3, 35328\ns_lshl_b32 s58, s16, 10\ns_add_u32 s58, s58, s57\n"
"v_cmp_eq_u32 vcc, s17, v121\nv_bfrev_b32 v123, 1\ns_nop 0\nv_cndmask_b32 v23, v123, v127, vcc\nv_add_u32 v123, 141312, v6\nG 144,9,128\nG 148,10,128\nG 152,11,128\n"
"s_waitcnt vmcnt(12)\nds_write_b128 v254, v[200:203] offset:0\nds_read_b64_tr_b16 v[216:217], v252 offset:0\nds_read_b64_tr_b16 v[218:219], v252 offset:512\ns_waitcnt vmcnt(11)\n"
"ds_write_b128 v254, v[204:207] offset:1024\nds_read_b64_tr_b16 v[220:221], v252 offset:1024\nds_read_b64_tr_b16 v[222:223], v252 offset:1536\ns_waitcnt vmcnt(10)\n"
"ds_write_b128 v254, v[208:211] offset:2048\nds_read_b64_tr_b16 v[224:225], v252 offset:2048\nds_read_b64_tr_b16 v[226:227], v252 offset:2560\ns_waitcnt vmcnt(9)\n"
"ds_write_b128 v254, v[212:215] offset:3072\nds_read_b64_tr_b16 v[228:229], v252 offset:3072\nds_read_b64_tr_b16 v[230:231], v252 offset:3584\ns_waitcnt lgkmcnt(10)\nL 64,216\n"
"A 65,216\nL 66,217\nA 67,217\ns_waitcnt lgkmcnt(9)\nL 68,218\nA 69,218\nL 70,219\nA 71,219\ns_waitcnt lgkmcnt(7)\nL 72,220\nA 73,220\nL 74,221\nA 75,221\ns_waitcnt lgkmcnt(6)\n"
"L 76,222\nA 77,222\nL 78,223\nA 79,223\nv_pk_add_f32 v[96:97], v[64:65], v[72:73]\nv_pk_add_f32 v[98:99], v[66:67], v[74:75]\nv_pk_add_f32 v[100:101], v[68:69], v[76:77]\n"
"v_pk_add_f32 v[102:103], v[70:71], v[78:79]\ns_waitcnt lgkmcnt(4)\nL 80,224\nA 81,224\nL 82,225\nA 83,225\ns_waitcnt lgkmcnt(3)\nL 84,226\nA 85,226\nL 86,227\nA 87,227\n"
"s_waitcnt lgkmcnt(1)\nL 88,228\nA 89,228\nL 90,229\nA 91,229\ns_waitcnt lgkmcnt(0)\nL 92,230\nA 93,230\nL 94,231\nA 95,231\nv_pk_add_f32 v[104:105], v[80:81], v[88:89]\n"
"v_pk_add_f32 v[106:107], v[82:83], v[90:91]\nv_pk_add_f32 v[108:109], v[84:85], v[92:93]\nv_pk_add_f32 v[110:111], v[86:87], v[94:95]\nv_pk_add_f32 v[112:113], v[96:97], v[104:105]\n"
"v_pk_add_f32 v[114:115], v[98:99], v[106:107]\nv_pk_add_f32 v[116:117], v[100:101], v[108:109]\nv_pk_add_f32 v[118:119], v[102:103], v[110:111]\nQ2 112,112\nQ2 114,114\nQ2 116,116\n"
"Q2 118,118\nC 248,112,113\nC 249,114,115\nC 250,116,117\nC 251,118,119\nglobal_store_dwordx4 v198, v[248:251], s[44:45] sc1\nv_add_u32 v198, 0x10000, v198\ns_barrier\n"
"buffer_load_dword v197, v17, s[28:31], 0 offen\nQ2 96,96\nQ2 98,98\nQ2 100,100\nQ2 102,102\nQ2 104,104\nQ2 106,106\nQ2 108,108\nQ2 110,110\nC 240,96,97\nC 241,98,99\nC 242,100,101\n"
"C 243,102,103\nC 244,104,105\nC 245,106,107\nC 246,108,109\nC 247,110,111\nv_pk_add_f32 v[96:97], v[64:65], v[80:81]\nv_pk_add_f32 v[98:99], v[66:67], v[82:83]\n"
"v_pk_add_f32 v[100:101], v[68:69], v[84:85]\nv_pk_add_f32 v[102:103], v[70:71], v[86:87]\nv_pk_add_f32 v[104:105], v[72:73], v[88:89]\nv_pk_add_f32 v[106:107], v[74:75], v[90:91]\n"
"v_pk_add_f32 v[108:109], v[76:77], v[92:93]\nv_pk_add_f32 v[110:111], v[78:79], v[94:95]\nQ2 96,96\nQ2 98,98\nQ2 100,100\nQ2 102,102\nQ2 104,104\nQ2 106,106\nQ2 108,108\nQ2 110,110\n"
"C 236,96,97\nC 237,98,99\nC 238,100,101\nC 239,102,103\nC 232,104,105\nC 233,106,107\nC 234,108,109\nC 235,110,111\nQ2 64,64\nQ2 66,66\nQ2 68,68\nQ2 70,70\nC 216,64,65\nC 217,66,67\n"
"C 218,68,69\nC 219,70,71\nQ2 72,72\nQ2 74,74\nQ2 76,76\nQ2 78,78\nC 220,72,73\nC 221,74,75\nC 222,76,77\nC 223,78,79\nQ2 80,80\nQ2 82,82\nQ2 84,84\nQ2 86,86\nC 224,80,81\n"
"C 225,82,83\nC 226,84,85\nC 227,86,87\nQ2 88,88\nQ2 90,90\nQ2 92,92\nQ2 94,94\nC 228,88,89\nC 229,90,91\nC 230,92,93\nC 231,94,95\ns_waitcnt vmcnt(10)\nv_add_u32 v17, 1, v124\n"
"s_waitcnt vmcnt(1)\ns_barrier\nbuffer_store_dword v17, v23, s[40:43], 0 offen offset:0 sc1\nG 156,12,128\nH 88,13,3200\nG 160,9,256\nG 164,10,256\nG 168,11,256\nS 3,128,0\n"
"S 3,132,256\nS 3,136,512\nS 3,140,768\nU 6,20,0\nS 3,144,17664\nS 3,148,17920\nS 3,152,18176\ns_mov_b64 exec, 0\ns_cmp_eq_u32 s16, 1\ns_cbranch_scc1 .Lpad0\ns_cmp_eq_u32 s16, 2\n"
"s_cbranch_scc1 .Lpad1\ns_cmp_eq_u32 s16, 3\ns_cbranch_scc1 .Lpad2\ns_branch .Lpad3\n.Lret1:\ns_mov_b64 exec, -1\nG 172,12,256\nH 89,13,3328\nG 176,9,384\ns_waitcnt vmcnt(7)\n"
"S 3,156,18432\nG 180,10,384\ns_waitcnt vmcnt(7)\nU 6,88,17664\nG 184,11,384\ns_waitcnt vmcnt(7)\nS 3,160,35328\nG 188,12,384\ns_waitcnt vmcnt(7)\nS 3,164,35584\nH 90,13,3456\n"
"s_waitcnt vmcnt(7)\nS 3,168,35840\nG 24,8,3072\ns_waitcnt vmcnt(7)\nS 3,172,36096\nG 28,9,1024\ns_waitcnt vmcnt(7)\nU 6,89,35328\nG 32,9,3072\nG 36,10,1024\nG 40,10,3072\n"
"G 44,11,1024\nG 48,11,3072\nG 52,12,1024\nH 19,13,2048\nH 21,14,0\ns_cmp_lg_u32 s16, 0\ns_cbranch_scc1 2f\nbuffer_load_dword v196, v127, s[40:43], 0 offen sc1\n2:\n"
"s_cmp_lg_u32 s16, 0\ns_cbranch_scc1 2f\ns_mov_b32 s47, 0x400000\n1:\ns_waitcnt vmcnt(0)\nv_cmp_eq_u32 vcc, v196, v124\ns_cbranch_vccz 2f\n"
"buffer_load_dword v196, v127, s[40:43], 0 offen sc1\ns_sub_u32 s47, s47, 1\ns_cbranch_scc0 1b\n2:\ns_waitcnt lgkmcnt(0)\ns_barrier\ns_cmp_eq_u32 s17, 63\ns_cbranch_scc1 .Lidle\n"
"K 160,-4096\nK 164,0\nD 128,0,0\nD 132,0,256\nD 136,0,512\nD 140,0,768\nD 144,0,1024\nD 148,0,1280\nD 152,0,1536\nD 156,0,1792\nK 168,-3072\nK 172,1024\nK 176,-2048\nK 180,2048\n"
"K 184,-1024\nK 188,3072\nv_add_u32 v15, 0x2000, v15\nK 192,-4096\nK 196,-3072\nK 200,-2048\nK 204,-1024\nL 104,128\ns_waitcnt vmcnt(21)\nL 107,24\nY 107,107,104\n"
"s_waitcnt vmcnt(20)\nL 108,28\nY 108,108,104\nA 114,128\nA 117,24\nY 117,117,114\nA 118,28\nY 118,118,114\nC 64,108,118\nC 68,107,117\nL 104,129\nL 107,25\nY 107,107,104\nL 108,29\n"
"Y 108,108,104\nA 114,129\nA 117,25\nY 117,117,114\nA 118,29\nY 118,118,114\nC 65,108,118\nC 69,107,117\nL 104,130\nL 107,26\nY 107,107,104\nL 108,30\nY 108,108,104\nA 114,130\n"
"A 117,26\nY 117,117,114\nA 118,30\nY 118,118,114\nC 66,108,118\nC 70,107,117\nL 104,131\nL 107,27\nY 107,107,104\nL 108,31\ns_waitcnt vmcnt(11) lgkmcnt(7)\nMZ 0,160,128\n"
"s_waitcnt lgkmcnt(6)\nK 208,3072\nv_add_u32 v17, 3, v124\nMZ 8,160,132\ns_waitcnt lgkmcnt(5)\nMZ 16,160,136\ns_waitcnt lgkmcnt(4)\nMZ 24,160,140\ns_waitcnt lgkmcnt(3)\n"
"MZ 32,160,144\ns_waitcnt lgkmcnt(2)\nK 212,0\nMZ 40,160,148\ns_waitcnt lgkmcnt(1)\nMZ 48,160,152\ns_waitcnt lgkmcnt(0)\nMZ 56,160,156\ns_waitcnt vmcnt(12)\nMZ 4,164,128\n"
"D 128,0,2080\nMZ 12,164,132\nK 216,1024\nMZ 20,164,136\nMZ 28,164,140\nMZ 36,164,144\nMZ 44,164,148\nK 220,2048\nv_add_u32 v15, 0x2000, v15\nMZ 52,164,152\nMZ 60,164,156\n"
"s_waitcnt vmcnt(13)\nM 0,168,132\nM 8,168,136\nbuffer_store_dword v17, v23, s[40:43], 0 offen sc1\nM 16,168,140\nM 24,168,144\nM 32,168,148\nM 40,168,152\n"
"global_store_dwordx4 v198, v[232:235], s[44:45] sc1\nM 48,168,156\ns_waitcnt lgkmcnt(0)\nv_add_u32 v198, 0x10000, v198\nM 56,168,128\ns_waitcnt vmcnt(14)\nM 4,172,132\nD 132,0,2336\n"
"M 12,172,136\nK 224,-4096\nM 20,172,140\nM 28,172,144\nM 36,172,148\nM 44,172,152\nK 228,-3072\nM 52,172,156\nM 60,172,128\ns_waitcnt vmcnt(15)\nM 0,176,136\nM 8,176,140\n"
"K 232,-2048\nM 16,176,144\nM 24,176,148\nM 32,176,152\nM 40,176,156\nK 236,-1024\nM 48,176,128\ns_waitcnt lgkmcnt(0)\nM 56,176,132\ns_waitcnt vmcnt(16)\nM 4,180,136\nD 136,0,2592\n"
"Y 108,108,104\nM 12,180,140\nK 240,3072\nM 20,180,144\nA 114,131\nM 28,180,148\nM 36,180,152\nA 117,27\nM 44,180,156\nK 244,0\nM 52,180,128\nY 117,117,114\nM 60,180,132\n"
"s_waitcnt vmcnt(17)\ns_branch .Lpad0\n.Lidle:\nglobal_store_dwordx4 v198, v[232:235], s[44:45] sc1\nv_add_u32 v198, 0x10000, v198\n"
"global_store_dwordx4 v198, v[236:239], s[44:45] sc1\nv_add_u32 v198, 0x10000, v198\nglobal_store_dwordx4 v198, v[240:243], s[44:45] sc1\nv_add_u32 v198, 0x10000, v198\n"
"global_store_dwordx4 v198, v[244:247], s[44:45] sc1\nv_add_u32 v198, 0x10000, v198\nglobal_store_dwordx4 v198, v[216:219], s[44:45] sc1\nv_add_u32 v198, 0x10000, v198\n"
"global_store_dwordx4 v198, v[220:223], s[44:45] sc1\nv_add_u32 v198, 0x10000, v198\nglobal_store_dwordx4 v198, v[224:227], s[44:45] sc1\nv_add_u32 v198, 0x10000, v198\n"
"global_store_dwordx4 v198, v[228:231], s[44:45] sc1\nv_add_u32 v124, 3, v124\nv_add_u32 v125, 1, v125\nv_add_u32 v126, 1, v126\ns_waitcnt vmcnt(0)\ns_barrier\n"
"buffer_store_dwordx3 v[124:126], v23, s[40:43], 0 offen sc1\ns_endpgm\n.p2align 12\n.Lbase:\n.Lpad0:\n.if (.Lpad0 - .Lbase) != 0\n.error \"pad\"\n.endif\ns_cbranch_execz .Lpad4\n"
"M 0,184,140\nA 118,31\nM 8,184,144\nK 248,1024\nM 16,184,148\nY 118,118,114\nM 24,184,152\nM 32,184,156\nC 67,108,118\nM 40,184,128\nK 252,2048\nv_add_u32 v15, 0x2000, v15\n"
"M 48,184,132\ns_waitcnt lgkmcnt(0)\nC 71,107,117\nM 56,184,136\ns_waitcnt vmcnt(18)\nM 4,188,140\nD 140,0,64\nM 12,188,144\nD 144,0,320\nM 20,188,148\nD 148,0,576\nL 104,132\n"
"M 28,188,152\nD 152,0,832\nM 36,188,156\nD 156,0,1088\nL 107,32\nM 44,188,128\nD 128,0,1344\nM 52,188,132\nD 132,0,1600\nY 107,107,104\nM 60,188,136\ns_waitcnt vmcnt(17) lgkmcnt(6)\n"
"D 136,0,1856\nM 0,192,140\ns_waitcnt vmcnt(16) lgkmcnt(6)\nglobal_store_dwordx4 v198, v[236:239], s[44:45] sc1\nM 0,196,144\ns_waitcnt vmcnt(16) lgkmcnt(5)\nS 4,176,0\nM 0,200,148\n"
"s_waitcnt vmcnt(15) lgkmcnt(5)\nv_add_u32 v198, 0x10000, v198\nL 108,36\nM 0,204,152\ns_waitcnt vmcnt(14)\nY 108,108,104\nM 4,208,152\ns_waitcnt vmcnt(13)\nK 160,-4096\nM 4,212,140\n"
"s_waitcnt vmcnt(13)\nD 140,0,2144\nM 4,216,144\ns_waitcnt vmcnt(12)\nS 4,180,256\nM 4,220,148\nA 114,132\nM 8,196,148\nK 164,-3072\nA 117,32\nM 8,200,152\ns_waitcnt lgkmcnt(6)\n"
"S 4,184,512\nM 8,204,156\nY 117,117,114\nM 8,192,144\nM 12,212,144\nD 144,0,2400\nA 118,36\nM 12,216,148\nK 168,-2048\nM 12,220,152\nS 4,188,768\nM 12,208,156\nU 6,90,52992\n"
"M 16,200,156\nY 118,118,114\nM 16,196,152\ns_waitcnt lgkmcnt(9)\nK 172,-1024\nM 16,204,128\nS 4,64,17664\nM 16,192,148\nC 64,108,118\nM 20,212,148\nM 20,216,152\nD 148,0,2656\n"
"M 20,220,156\nK 176,3072\nM 20,208,128\nS 4,68,35328\nM 24,200,128\nC 68,107,117\nL 104,133\nM 24,196,156\ns_waitcnt lgkmcnt(11)\nL 107,33\nY 107,107,104\nM 24,204,132\nK 180,0\n"
"L 108,37\nM 24,192,152\nM 28,212,152\nD 152,0,17664\nY 108,108,104\nM 28,216,156\nM 28,220,128\nglobal_store_dwordx4 v198, v[240:243], s[44:45] sc1\nM 28,208,132\n"
"v_add_u32 v198, 0x10000, v198\nA 114,133\nM 32,200,132\nA 117,33\nM 32,196,128\ns_waitcnt lgkmcnt(11)\nM 32,204,136\nK 184,1024\nY 117,117,114\nM 32,192,156\nM 36,212,156\n"
"D 156,0,17920\nA 118,37\nM 36,216,128\nM 36,220,132\nK 188,2048\nv_add_u32 v15, 0x2000, v15\nM 36,208,136\nY 118,118,114\nM 40,200,136\nC 65,108,118\nM 40,196,132\n"
"s_waitcnt lgkmcnt(10)\nM 40,204,140\nC 69,107,117\nM 40,192,128\nM 44,212,128\nD 128,0,18176\nL 104,134\nM 44,216,132\nM 44,220,136\nL 107,34\nM 44,208,140\nM 48,200,140\n"
"Y 107,107,104\nM 48,196,136\ns_waitcnt lgkmcnt(8)\nM 48,204,144\nL 108,38\nM 48,192,132\nM 52,212,132\nD 132,0,18432\nY 108,108,104\nM 52,216,136\nM 52,220,140\nA 114,134\n"
"M 52,208,144\nM 56,200,144\nK 200,-4096\nA 117,34\nM 56,196,140\ns_waitcnt lgkmcnt(5)\nM 56,204,148\nY 117,117,114\nM 56,192,136\nM 60,212,136\nD 136,0,18688\nA 118,38\n"
"M 60,216,140\nD 140,0,18944\nM 60,220,144\nD 144,0,19200\nY 118,118,114\nM 60,208,148\ns_waitcnt vmcnt(18) lgkmcnt(6)\nD 148,0,19456\nM 0,224,152\ns_waitcnt vmcnt(17) lgkmcnt(6)\n"
"global_store_dwordx4 v198, v[244:247], s[44:45] sc1\nM 0,228,156\ns_waitcnt vmcnt(17) lgkmcnt(5)\nv_add_u32 v198, 0x10000, v198\nv_add_u32 v17, 1, v125\nM 0,232,128\n"
"s_waitcnt vmcnt(16) lgkmcnt(4)\nC 66,108,118\nC 70,107,117\nM 0,236,132\ns_waitcnt vmcnt(15)\nM 4,240,132\ns_waitcnt vmcnt(14)\nK 196,-3072\nL 104,135\nM 4,244,152\n"
"s_waitcnt vmcnt(14)\nD 152,0,19744\nM 4,248,156\ns_waitcnt vmcnt(13)\nL 107,35\nM 4,252,128\nM 8,228,128\nK 204,-2048\nY 107,107,104\nM 8,232,132\ns_waitcnt lgkmcnt(4)\nM 8,236,136\n"
"L 108,39\nM 8,224,156\nM 12,244,156\nD 156,0,20000\nY 108,108,104\nM 12,248,128\nK 192,-1024\nM 12,252,132\nA 114,135\nM 12,240,136\nM 16,232,136\nA 117,35\nM 16,228,132\n"
"s_waitcnt lgkmcnt(4)\nK 212,3072\nM 16,236,140\nY 117,117,114\nM 16,224,128\nM 20,244,128\nD 128,0,20256\nA 118,39\nM 20,248,132\nK 216,0\nM 20,252,136\nY 118,118,114\nM 20,240,140\n"
"M 24,232,140\nC 67,108,118\nM 24,228,136\ns_waitcnt lgkmcnt(4)\nK 220,1024\nM 24,236,144\nC 71,107,117\nM 24,224,132\nM 28,244,132\nD 132,0,17728\nM 28,248,136\nK 208,2048\n"
"v_add_u32 v15, 0x2000, v15\nM 28,252,140\nS 4,64,17920\nM 28,240,144\nL 104,136\nM 32,232,144\nL 107,40\nM 32,228,140\ns_waitcnt lgkmcnt(5)\nM 32,236,148\nY 107,107,104\n"
"M 32,224,136\nM 36,244,136\nD 136,0,17984\nL 108,44\nM 36,248,140\nM 36,252,144\nY 108,108,104\nM 36,240,148\nM 40,232,148\nS 4,68,35584\nM 40,228,144\ns_waitcnt lgkmcnt(6)\n"
"M 40,236,152\nA 114,136\nM 40,224,140\nM 44,244,140\nD 140,0,18240\nA 117,40\nM 44,248,144\nM 44,252,148\nY 117,117,114\nM 44,240,152\nM 48,232,152\nA 118,44\nM 48,228,148\n"
"s_waitcnt lgkmcnt(6)\nM 48,236,156\nY 118,118,114\nM 48,224,144\nM 52,244,144\nD 144,0,18496\nC 64,108,118\nM 52,248,148\nM 52,252,152\nC 68,107,117\nM 52,240,156\nM 56,232,156\n"
"K 232,-4096\nL 104,137\nM 56,228,152\ns_waitcnt lgkmcnt(6)\nM 56,236,128\nL 107,41\nM 56,224,148\nM 60,244,148\nD 148,0,18752\nY 107,107,104\nM 60,248,152\nD 152,0,19008\n"
"M 60,252,156\nD 156,0,19264\nL 108,45\nM 60,240,128\ns_waitcnt vmcnt(18) lgkmcnt(8)\nD 128,0,19520\nM 0,160,132\ns_waitcnt vmcnt(17) lgkmcnt(7)\nK 228,-3072\nY 108,108,104\n"
"M 0,164,136\ns_waitcnt vmcnt(17) lgkmcnt(5)\nM 0,168,140\ns_waitcnt vmcnt(16) lgkmcnt(4)\nA 114,137\nM 0,172,144\ns_waitcnt vmcnt(15)\nM 4,176,144\ns_waitcnt vmcnt(14)\nK 236,-2048\n"
"A 117,41\nM 4,180,132\ns_waitcnt vmcnt(13)\nD 132,0,19808\nM 4,184,136\ns_waitcnt vmcnt(12)\nY 117,117,114\nM 4,188,140\nM 8,164,140\nK 224,-1024\nA 118,45\nM 8,168,144\n"
"s_waitcnt lgkmcnt(4)\nM 8,172,148\nY 118,118,114\nM 8,160,136\nM 12,180,136\nD 136,0,20064\nC 65,108,118\nM 12,184,140\nK 244,3072\nM 12,188,144\nC 69,107,117\nM 12,176,148\n"
"M 16,168,148\nL 104,138\nM 16,164,144\ns_waitcnt lgkmcnt(4)\nK 248,0\nM 16,172,152\nL 107,42\nM 16,160,140\nM 20,180,140\nD 140,0,20320\nY 107,107,104\nM 20,184,144\nK 252,1024\n"
"M 20,188,148\nM 20,176,152\nL 108,46\nM 24,168,152\ns_waitcnt vmcnt(14) lgkmcnt(0)\ns_barrier\nM 24,164,148\nbuffer_store_dword v17, v23, s[40:43], 0 offen offset:4 sc1\n"
"M 24,172,156\nY 108,108,104\nM 24,160,144\nA 114,138\nM 28,180,144\nD 144,0,35328\nM 28,184,148\nK 240,2048\nv_add_u32 v15, 0x2000, v15\nM 28,188,152\nA 117,42\nM 28,176,156\n"
"Y 117,117,114\nM 32,168,156\nM 32,164,152\nbuffer_load_dword v193, v199, s[28:31], 0 offen offset:128\nM 32,172,128\nA 118,46\nM 32,160,148\nY 118,118,114\nM 36,180,148\n"
"D 148,0,35584\nM 36,184,152\nC 66,108,118\nM 36,188,156\nM 36,176,128\nC 70,107,117\nM 40,168,128\nM 40,164,156\nL 104,139\nM 40,172,132\nM 40,160,152\nL 107,43\nM 44,180,152\n"
"D 152,0,35840\nM 44,184,156\nY 107,107,104\nM 44,188,128\nM 44,176,132\nL 108,47\nM 48,168,132\nM 48,164,128\nY 108,108,104\nM 48,172,136\nM 48,160,156\nA 114,139\nM 52,180,156\n"
"D 156,0,36096\nM 52,184,128\nA 117,43\nM 52,188,132\nM 52,176,136\nY 117,117,114\nM 56,168,136\nK 168,-4096\nM 56,164,132\nA 118,47\nM 56,172,140\nM 56,160,128\nY 118,118,114\n"
"M 60,180,128\nD 128,0,36352\nM 60,184,132\nD 132,0,36608\nC 67,108,118\nM 60,188,136\nD 136,0,36864\nM 60,176,140\ns_waitcnt lgkmcnt(6)\nD 140,0,37120\nC 71,107,117\nM 0,200,144\n"
"s_waitcnt vmcnt(17) lgkmcnt(6)\nK 164,-3072\nM 0,196,148\ns_waitcnt vmcnt(17) lgkmcnt(5)\nS 4,64,18176\nM 0,204,152\ns_waitcnt vmcnt(16) lgkmcnt(5)\nM 0,192,156\n"
"s_waitcnt vmcnt(15)\nL 104,140\nM 4,212,156\ns_waitcnt vmcnt(14)\nK 172,-2048\nM 4,216,144\ns_waitcnt vmcnt(14)\nD 144,0,37408\nL 107,48\nM 4,220,148\ns_waitcnt vmcnt(13)\n"
"M 4,208,152\nY 107,107,104\nM 8,196,152\nK 160,-1024\nM 8,204,156\ns_waitcnt lgkmcnt(5)\nL 108,52\nM 8,192,128\nM 8,200,148\nY 108,108,104\nM 12,216,148\nD 148,0,37664\n"
"M 12,220,152\nK 180,3072\nM 12,208,156\nS 4,68,35840\nM 12,212,128\nA 114,140\nM 16,204,128\nM 16,196,156\ns_waitcnt lgkmcnt(6)\nK 184,0\nA 117,48\nM 16,192,132\nM 16,200,152\n"
"Y 117,117,114\nM 20,216,152\nD 152,0,37920\nM 20,220,156\nK 188,1024\nA 118,52\nM 20,208,128\nM 20,212,132\nY 118,118,114\nM 24,204,132\nM 24,196,128\ns_waitcnt lgkmcnt(6)\n"
"K 176,2048\nv_add_u32 v15, 0x2000, v15\nM 24,192,136\nC 64,108,118\nM 24,200,156\nC 68,107,117\nM 28,216,156\nD 156,0,35392\nM 28,220,128\nL 104,141\nM 28,208,132\nM 28,212,136\n"
"L 107,49\nM 32,204,136\nM 32,196,132\ns_waitcnt lgkmcnt(6)\nY 107,107,104\nM 32,192,140\nM 32,200,128\nL 108,53\nM 36,216,128\nD 128,0,35648\nM 36,220,132\nY 108,108,104\n"
"M 36,208,136\nM 36,212,140\nA 114,141\nM 40,204,140\nM 40,196,136\ns_waitcnt lgkmcnt(5)\nA 117,49\nM 40,192,144\nM 40,200,132\nY 117,117,114\nM 44,216,132\nD 132,0,35904\n"
"M 44,220,136\nA 118,53\nM 44,208,140\nM 44,212,144\nY 118,118,114\nM 48,204,144\nM 48,196,140\ns_waitcnt lgkmcnt(5)\nC 65,108,118\nM 48,192,148\nM 48,200,136\nC 69,107,117\n"
"M 52,216,136\nD 136,0,36160\nM 52,220,140\nL 104,142\nM 52,208,144\nbuffer_load_dword v124, v127, s[40:43], 0 offen offset:4 sc1\nM 52,212,148\nL 107,50\nM 56,204,148\nM 56,196,144\n"
"s_waitcnt lgkmcnt(4)\nY 107,107,104\nM 56,192,152\nK 204,-4096\nM 56,200,140\nL 108,54\nM 60,216,140\nD 140,0,36416\nM 60,220,144\nD 144,0,36672\nY 108,108,104\nM 60,208,148\n"
"D 148,0,36928\nM 60,212,152\ns_waitcnt vmcnt(19) lgkmcnt(6)\nD 152,0,37184\nA 114,142\nM 0,232,156\ns_waitcnt vmcnt(18) lgkmcnt(6)\nK 196,-3072\nM 0,228,128\n"
"s_waitcnt vmcnt(18) lgkmcnt(5)\nA 117,50\nM 0,236,132\ns_waitcnt vmcnt(17) lgkmcnt(4)\nM 0,224,136\ns_waitcnt vmcnt(16)\nY 117,117,114\nM 4,244,136\ns_waitcnt vmcnt(15)\n"
"K 192,-2048\nM 4,248,156\ns_waitcnt vmcnt(15)\nD 156,0,37472\nA 118,54\nM 4,252,128\ns_waitcnt vmcnt(13)\nM 4,240,132\nY 118,118,114\nM 8,228,132\nG 56,8,3200\nM 8,236,136\n"
"s_waitcnt lgkmcnt(4)\nC 66,108,118\nM 8,224,140\n.Lpad1:\n.if (.Lpad1 - .Lbase) != 4104\n.error \"pad\"\n.endif\ns_cbranch_execz .Lpad5\nM 8,232,128\nC 70,107,117\nM 12,248,128\n"
"D 128,0,37728\nM 12,252,132\nG 60,9,1152\nM 12,240,136\nL 104,143\nM 12,244,140\nL 107,51\nM 16,236,140\nM 16,228,136\ns_waitcnt lgkmcnt(4)\nG 24,9,3200\nM 16,224,144\n"
"Y 107,107,104\nM 16,232,132\nL 108,55\nM 20,248,132\nD 132,0,37984\nM 20,252,136\nG 28,10,1152\nM 20,240,140\nY 108,108,104\nM 20,244,144\nA 114,143\nM 24,236,144\nM 24,228,140\n"
"s_waitcnt lgkmcnt(4)\nK 200,-1024\nA 117,51\nM 24,224,148\nM 24,232,136\nY 117,117,114\nM 28,248,136\nD 136,1,0\nM 28,252,140\nK 216,3072\nA 118,55\nM 28,240,144\nM 28,244,148\n"
"Y 118,118,114\nM 32,236,148\nM 32,228,144\ns_waitcnt lgkmcnt(4)\nK 220,0\nC 67,108,118\nM 32,224,152\nM 32,232,140\nC 71,107,117\nM 36,248,140\nD 140,1,256\nM 36,252,144\n"
"K 208,1024\nM 36,240,148\nS 4,64,18432\nM 36,244,152\nL 104,20\nM 40,236,152\nM 40,228,148\ns_waitcnt lgkmcnt(5)\nK 212,2048\nv_add_u32 v15, 0x2000, v15\nM 40,224,156\nL 106,19\n"
"M 40,232,144\nY 106,106,104\nM 44,248,144\nD 144,1,512\nM 44,252,148\nG 32,10,3200\nM 44,240,152\nL 105,21\nM 44,244,156\nY 105,105,104\nM 48,236,156\nM 48,228,152\n"
"s_waitcnt lgkmcnt(5)\nG 36,11,1152\nM 48,224,128\nS 4,68,36096\nM 48,232,148\nA 114,20\nM 52,248,148\nM 52,252,152\nD 148,1,768\nA 116,19\nM 52,240,156\nG 40,11,3200\nM 52,244,128\n"
"Y 116,116,114\nM 56,236,128\nM 56,228,156\ns_waitcnt lgkmcnt(6)\nA 115,21\nM 56,224,132\nG 44,12,1152\nM 56,232,152\nY 115,115,114\nM 60,248,152\nD 152,1,1024\nM 60,252,156\n"
"D 156,1,1280\nC 64,105,115\nM 60,240,128\nD 128,1,1536\nM 60,244,132\ns_waitcnt vmcnt(24) lgkmcnt(8)\nD 132,1,1792\nC 65,106,116\nM 0,168,136\ns_waitcnt vmcnt(23) lgkmcnt(8)\n"
"H 19,13,2176\nM 0,164,140\ns_waitcnt vmcnt(23) lgkmcnt(6)\nU 7,64,0\nM 0,172,144\ns_waitcnt vmcnt(22) lgkmcnt(5)\nH 21,14,128\nM 0,160,148\ns_waitcnt vmcnt(22)\nU 7,65,17664\n"
"M 4,180,148\ns_waitcnt vmcnt(21)\nM 4,184,136\ns_waitcnt vmcnt(20)\nD 136,1,2080\nM 4,188,140\ns_waitcnt vmcnt(19)\nL 104,144\nM 4,176,144\ns_waitcnt vmcnt(14)\nL 107,56\n"
"M 8,164,144\nY 107,107,104\nM 8,172,148\ns_waitcnt vmcnt(13) lgkmcnt(6)\nL 108,60\nM 8,160,152\ns_waitcnt lgkmcnt(0)\ns_barrier\nY 108,108,104\nM 8,168,140\nA 114,144\nM 12,184,140\n"
"D 140,1,2336\nA 117,56\nM 12,188,144\nY 117,117,114\nM 12,176,148\nA 118,60\nM 12,180,152\nY 118,118,114\nM 16,172,152\nv_cmp_eq_u32 vcc, v124, v125\ns_cbranch_vccz 2f\n"
"s_mov_b32 s47, 0x400000\n1:\nbuffer_load_dword v124, v127, s[40:43], 0 offen offset:4 sc1\ns_waitcnt vmcnt(0)\nv_cmp_eq_u32 vcc, v124, v125\ns_cbranch_vccz 2f\n"
"s_sub_u32 s47, s47, 1\ns_cbranch_scc0 1b\n2:\nM 16,164,148\nK 236,-4096\nC 64,108,118\nM 16,160,156\nC 68,107,117\nL 104,145\nM 16,168,144\nL 107,57\nM 20,184,144\nD 144,1,2592\n"
"M 20,188,148\nK 228,-3072\nY 107,107,104\nM 20,176,152\nL 108,61\nM 20,180,156\nY 108,108,104\nM 24,172,156\nA 114,145\nM 24,164,152\nK 224,-2048\nA 117,57\nM 24,160,128\n"
"Y 117,117,114\nM 24,168,148\nA 118,61\nM 28,184,148\nD 148,1,64\nY 118,118,114\nM 28,188,152\nK 232,-1024\nC 65,108,118\nM 28,176,156\nC 69,107,117\nM 28,180,128\nL 104,146\n"
"M 32,172,128\nL 107,58\nM 32,164,156\nK 248,3072\nY 107,107,104\nM 32,160,132\nM 32,168,152\nL 108,62\nM 36,184,152\nD 152,1,320\nY 108,108,104\nM 36,188,156\nK 252,0\nA 114,146\n"
"M 36,176,128\nA 117,58\nM 36,180,132\nY 117,117,114\nM 40,172,132\nA 118,62\nM 40,164,128\nK 240,1024\nY 118,118,114\nM 40,160,136\nC 66,108,118\nM 40,168,156\nC 70,107,117\n"
"M 44,184,156\nD 156,1,576\nL 104,147\nM 44,188,128\nK 244,2048\nv_add_u32 v15, 0x2000, v15\nM 44,176,132\nL 107,59\nY 107,107,104\nM 44,180,136\nL 108,63\nM 48,172,136\n"
"Y 108,108,104\nM 48,164,132\ns_waitcnt lgkmcnt(4)\nbuffer_load_dword v194, v199, s[28:31], 0 offen offset:256\nM 48,160,140\nA 114,147\nM 48,168,128\nA 117,59\nM 52,184,128\n"
"D 128,1,832\nY 117,117,114\nM 52,188,132\nA 118,63\nM 52,176,136\nY 118,118,114\nM 52,180,140\nC 67,108,118\nM 56,172,140\nK 172,-4096\nC 71,107,117\nM 56,164,136\n"
"s_waitcnt lgkmcnt(4)\nS 5,64,0\nM 56,160,144\nL 104,148\nM 56,168,132\ns_waitcnt vmcnt(22)\nL 107,24\nM 60,184,132\nD 132,1,1088\nY 107,107,104\nM 60,188,136\ns_waitcnt vmcnt(21)\n"
"D 136,1,1344\nL 108,28\nM 60,176,140\nD 140,1,1600\nY 108,108,104\nM 60,180,144\ns_waitcnt lgkmcnt(7)\nD 144,1,1856\nM 0,204,148\ns_waitcnt lgkmcnt(7)\nK 164,-3072\nM 0,196,152\n"
"s_waitcnt lgkmcnt(6)\nS 5,68,17664\nM 0,192,156\ns_waitcnt vmcnt(21) lgkmcnt(6)\nA 114,148\nA 117,24\nM 0,200,128\ns_waitcnt vmcnt(20)\nY 117,117,114\nM 4,216,128\n"
"s_waitcnt vmcnt(19)\nK 160,-2048\nA 118,28\nM 4,220,148\ns_waitcnt vmcnt(19)\nD 148,1,2144\nY 118,118,114\nM 4,208,152\ns_waitcnt vmcnt(18)\nC 64,108,118\nM 4,212,156\nC 68,107,117\n"
"M 8,196,156\nK 168,-1024\nL 104,149\nM 8,192,128\ns_waitcnt lgkmcnt(5)\nL 107,25\nM 8,200,132\nY 107,107,104\nM 8,204,152\nL 108,29\nM 12,220,152\nD 152,1,2400\nY 108,108,104\n"
"M 12,208,156\nK 184,3072\nM 12,212,128\nA 114,149\nM 12,216,132\nA 117,25\nM 16,192,132\nY 117,117,114\nM 16,196,128\ns_waitcnt lgkmcnt(5)\nK 188,0\nA 118,29\nM 16,200,136\n"
"Y 118,118,114\nM 16,204,156\nC 65,108,118\nM 20,220,156\nD 156,1,2656\nC 69,107,117\nM 20,208,128\nK 176,1024\nL 104,150\nM 20,212,132\nL 107,26\nM 20,216,136\nY 107,107,104\n"
"M 24,192,136\nL 108,30\nM 24,196,132\ns_waitcnt lgkmcnt(5)\nK 180,2048\nv_add_u32 v15, 0xe000, v15\nM 24,200,140\nY 108,108,104\nA 114,150\nM 24,204,128\nA 117,26\nM 28,220,128\n"
"D 128,1,17664\nM 28,208,132\nY 117,117,114\nM 28,212,136\nA 118,30\nM 28,216,140\nY 118,118,114\nM 32,192,140\nC 66,108,118\nM 32,196,136\ns_waitcnt lgkmcnt(5)\nC 70,107,117\n"
"M 32,200,144\nL 104,151\nM 32,204,132\nL 107,27\nM 36,220,132\nD 132,1,17920\nY 107,107,104\nM 36,208,136\nL 108,31\nM 36,212,140\nY 108,108,104\nM 36,216,144\nA 114,151\n"
"M 40,192,144\nA 117,27\nM 40,196,140\ns_waitcnt lgkmcnt(4)\nY 117,117,114\nM 40,200,148\nM 40,204,136\nA 118,31\nM 44,220,136\nD 136,1,18176\nY 118,118,114\nM 44,208,140\n"
"C 67,108,118\nM 44,212,144\nC 71,107,117\nM 44,216,148\nS 5,64,256\nM 48,192,148\nL 104,152\nM 48,196,144\ns_waitcnt vmcnt(22) lgkmcnt(5)\nL 107,32\nM 48,200,152\nY 107,107,104\n"
"M 48,204,140\ns_waitcnt vmcnt(21)\nL 108,36\nM 52,220,140\nD 140,1,18432\nY 108,108,104\nM 52,208,144\nS 5,68,17920\nM 52,212,148\nA 114,152\nM 52,216,152\nA 117,32\nM 56,192,152\n"
"K 192,-4096\nM 56,196,148\ns_waitcnt lgkmcnt(6)\nY 117,117,114\nM 56,200,156\nA 118,36\nM 56,204,144\nY 118,118,114\nM 60,220,144\nD 144,1,18688\nC 64,108,118\nM 60,208,148\n"
"D 148,1,18944\nC 68,107,117\nM 60,212,152\nD 152,1,19200\nL 104,153\nM 60,216,156\ns_waitcnt vmcnt(17) lgkmcnt(8)\nD 156,1,19456\nL 107,33\nMC 64,236,128,0\n"
"s_waitcnt vmcnt(16) lgkmcnt(8)\nK 196,-3072\nY 107,107,104\nM 64,228,132\ns_waitcnt vmcnt(16) lgkmcnt(7)\nL 108,37\nM 64,224,136\ns_waitcnt vmcnt(15) lgkmcnt(5)\nY 108,108,104\n"
"M 64,232,140\ns_waitcnt vmcnt(14)\nA 114,153\nMC 68,248,140,4\ns_waitcnt vmcnt(13)\nK 200,-2048\nA 117,33\nM 68,252,128\ns_waitcnt vmcnt(13)\nD 128,1,19744\nY 117,117,114\n"
"M 68,240,132\ns_waitcnt vmcnt(12)\nA 118,37\nM 68,244,136\nMC 72,228,136,8\nK 204,-1024\nY 118,118,114\nM 72,224,140\ns_waitcnt lgkmcnt(4)\nC 65,108,118\nM 72,232,144\nC 69,107,117\n"
"M 72,236,132\nL 104,154\nMC 76,252,132,12\nD 132,1,20000\nL 107,34\nM 76,240,136\nK 220,3072\nY 107,107,104\nM 76,244,140\nL 108,38\nM 76,248,144\nY 108,108,104\nMC 80,224,144,16\n"
"A 114,154\nM 80,228,140\ns_waitcnt lgkmcnt(4)\nK 208,0\nA 117,34\nM 80,232,148\nY 117,117,114\nM 80,236,136\nA 118,38\nMC 84,252,136,20\nD 136,1,20256\nY 118,118,114\nM 84,240,140\n"
"G 48,8,3328\nM 84,244,144\nC 66,108,118\nM 84,248,148\nC 70,107,117\nMC 88,224,148,24\nL 104,155\nM 88,228,144\ns_waitcnt lgkmcnt(4)\nK 212,1024\nL 107,35\nM 88,232,152\n"
"Y 107,107,104\nM 88,236,140\nL 108,39\nMC 92,252,140,28\nD 140,1,17728\nY 108,108,104\nM 92,240,144\nK 216,2048\nv_add_u32 v15, 0x2000, v15\nM 92,244,148\nA 114,155\nA 117,35\n"
"M 92,248,152\nY 117,117,114\nMC 96,224,152,32\nA 118,39\nM 96,228,148\ns_waitcnt lgkmcnt(4)\nG 52,9,1280\nM 96,232,156\nY 118,118,114\nC 67,108,118\nM 96,236,144\nC 71,107,117\n"
"MC 100,252,144,36\nD 144,1,17984\nM 100,240,148\nG 56,9,3328\nM 100,244,152\nS 5,64,512\nM 100,248,156\nL 104,156\nL 107,40\nMC 104,224,156,40\nY 107,107,104\nM 104,228,152\n"
"s_waitcnt lgkmcnt(5)\nG 60,10,1280\nM 104,232,128\nL 108,44\nY 108,108,104\nM 104,236,148\nMC 108,252,148,44\nD 148,1,18240\nA 114,156\nM 108,240,152\nG 24,10,3328\nM 108,244,156\n"
"S 5,68,18176\nM 108,248,128\nA 117,40\nY 117,117,114\nMC 112,224,128,48\nA 118,44\nY 118,118,114\nM 112,228,156\ns_waitcnt lgkmcnt(6)\nG 28,11,1280\nM 112,232,132\nC 64,108,118\n"
"M 112,236,152\nC 68,107,117\nMC 116,252,152,52\nD 152,1,18496\nL 104,157\nM 116,240,156\nG 32,11,3328\nM 116,244,128\nL 107,41\nY 107,107,104\nM 116,248,132\nL 108,45\n"
"MC 120,224,132,56\nY 108,108,104\nM 120,228,128\ns_waitcnt lgkmcnt(6)\nG 36,12,1280\nM 120,232,136\nA 114,157\nA 117,41\nM 120,236,156\nY 117,117,114\nMC 124,252,156,60\n"
"D 156,1,18752\nA 118,45\nM 124,240,128\nD 128,1,19008\nY 118,118,114\nM 124,244,132\nD 132,1,19264\nC 65,108,118\nM 124,248,136\ns_waitcnt vmcnt(23) lgkmcnt(8)\nD 136,1,19520\n"
"C 69,107,117\nM 64,172,140\ns_waitcnt vmcnt(22) lgkmcnt(8)\nK 224,-4096\nM 64,164,144\ns_waitcnt vmcnt(22) lgkmcnt(6)\nL 104,158\nM 64,160,148\ns_waitcnt vmcnt(21) lgkmcnt(4)\n"
"L 107,42\nM 64,168,152\ns_waitcnt vmcnt(20)\nY 107,107,104\nM 68,184,152\ns_waitcnt vmcnt(19)\nK 228,-3072\nL 108,46\nM 68,188,140\ns_waitcnt vmcnt(19)\nD 140,1,19808\n"
"Y 108,108,104\nM 68,176,144\ns_waitcnt vmcnt(18)\nA 114,158\nM 68,180,148\n.Lpad2:\n.if (.Lpad2 - .Lbase) != 8204\n.error \"pad\"\n.endif\ns_cbranch_execz .Lpad6\nA 117,42\n"
"M 72,164,148\nK 232,-2048\nY 117,117,114\nM 72,160,152\ns_waitcnt lgkmcnt(4)\nA 118,46\nM 72,168,156\nY 118,118,114\nM 72,172,144\nC 66,108,118\nM 76,188,144\nD 144,1,20064\n"
"C 70,107,117\nM 76,176,148\nK 236,-1024\nL 104,159\nM 76,180,152\nL 107,43\nM 76,184,156\nM 80,160,156\nY 107,107,104\nM 80,164,152\ns_waitcnt lgkmcnt(4)\nK 252,3072\nL 108,47\n"
"M 80,168,128\nY 108,108,104\nM 80,172,148\nA 114,159\nM 84,188,148\nD 148,1,20320\nA 117,43\nM 84,176,152\nK 240,0\nY 117,117,114\nM 84,180,156\nA 118,47\nM 84,184,128\n"
"Y 118,118,114\nM 88,160,128\nC 67,108,118\nM 88,164,156\ns_waitcnt lgkmcnt(4)\nK 244,1024\nC 71,107,117\nM 88,168,132\nS 5,64,768\nM 88,172,152\nL 104,88\nM 92,188,152\nL 106,19\n"
"M 92,176,156\nD 152,1,35328\nM 92,180,128\nK 248,2048\nv_add_u32 v15, 0xffff2000, v15\nM 92,184,132\nY 106,106,104\nL 105,21\nM 96,160,132\nY 105,105,104\nM 96,164,128\n"
"s_waitcnt lgkmcnt(5)\nS 5,68,18432\nM 96,168,136\nbuffer_load_dword v195, v199, s[28:31], 0 offen offset:384\nM 96,172,156\nA 114,88\nA 116,19\nM 100,188,156\nD 156,1,35584\n"
"Y 116,116,114\nM 100,176,128\nA 115,21\nM 100,180,132\nH 19,13,2304\nM 100,184,136\nY 115,115,114\nC 64,105,115\nM 104,160,136\nH 21,14,256\nM 104,164,132\ns_waitcnt lgkmcnt(6)\n"
"C 65,106,116\nM 104,168,140\nU 7,64,35328\nM 104,172,128\nU 7,65,52992\nM 108,188,128\nD 128,1,35840\nL 104,160\nM 108,176,132\ns_waitcnt vmcnt(20)\nL 107,48\nM 108,180,136\n"
"Y 107,107,104\nM 108,184,140\ns_waitcnt vmcnt(17)\nL 108,52\nM 112,160,140\nY 108,108,104\nM 112,164,136\ns_waitcnt lgkmcnt(0)\ns_barrier\nA 114,160\nM 112,168,144\nA 117,48\n"
"M 112,172,132\nY 117,117,114\nM 116,188,132\nD 132,1,36096\nA 118,52\nM 116,176,136\nY 118,118,114\nM 116,180,140\nC 64,108,118\nM 116,184,144\nC 68,107,117\nM 120,160,144\n"
"K 160,-4096\nL 104,161\nM 120,164,140\nL 107,49\nM 120,168,148\nM 120,172,136\nY 107,107,104\nM 124,188,136\nD 136,1,36352\nL 108,53\nM 124,176,140\nD 140,1,36608\nY 108,108,104\n"
"M 124,180,144\nD 144,1,36864\nA 114,161\nM 124,184,148\nD 148,1,37120\nA 117,49\nM 0,192,152\nK 164,-3072\nY 117,117,114\nM 0,196,156\nA 118,53\nM 0,200,128\ns_waitcnt lgkmcnt(4)\n"
"Y 118,118,114\nM 0,204,132\nC 65,108,118\nM 4,220,132\nK 168,-2048\nC 69,107,117\nM 4,208,152\nD 152,1,37408\nL 104,162\nM 4,212,156\nL 107,50\nM 4,216,128\nY 107,107,104\n"
"M 8,196,128\nK 172,-1024\nM 8,200,132\ns_waitcnt lgkmcnt(4)\nL 108,54\nM 8,204,136\nY 108,108,104\nM 8,192,156\nA 114,162\nM 12,208,156\nD 156,1,37664\nA 117,50\nM 12,212,128\n"
"K 188,3072\nY 117,117,114\nM 12,216,132\nA 118,54\nM 12,220,136\nY 118,118,114\nM 16,200,136\nC 66,108,118\nM 16,196,132\ns_waitcnt lgkmcnt(4)\nK 176,0\nC 70,107,117\nM 16,204,140\n"
"L 104,163\nM 16,192,128\nL 107,51\nM 20,208,128\nD 128,1,37920\nY 107,107,104\nM 20,212,132\nK 180,1024\nL 108,55\nM 20,216,136\nY 108,108,104\nM 20,220,140\nM 24,200,140\n"
"A 114,163\nM 24,196,136\ns_waitcnt lgkmcnt(4)\nK 184,2048\nv_add_u32 v15, 0x2000, v15\nM 24,204,144\nA 117,51\nY 117,117,114\nM 24,192,132\nA 118,55\nM 28,208,132\nD 132,1,35392\n"
"Y 118,118,114\nM 28,212,136\nC 67,108,118\nM 28,216,140\nC 71,107,117\nM 28,220,144\nS 3,64,0\nM 32,200,144\nL 104,164\nM 32,196,140\ns_waitcnt vmcnt(24) lgkmcnt(5)\nL 107,56\n"
"M 32,204,148\nY 107,107,104\nM 32,192,136\ns_waitcnt vmcnt(23)\nL 108,60\nM 36,208,136\nD 136,1,35648\nY 108,108,104\nM 36,212,140\nM 36,216,144\nS 3,68,17664\nM 36,220,148\n"
"A 114,164\nM 40,200,148\nA 117,56\nM 40,196,144\ns_waitcnt lgkmcnt(6)\nY 117,117,114\nM 40,204,152\nA 118,60\nM 40,192,140\nY 118,118,114\nM 44,208,140\nD 140,1,35904\nC 64,108,118\n"
"M 44,212,144\nC 68,107,117\nM 44,216,148\nL 104,165\nM 44,220,152\nL 107,57\nM 48,200,152\nY 107,107,104\nM 48,196,148\ns_waitcnt lgkmcnt(6)\nL 108,61\nM 48,204,156\nY 108,108,104\n"
"M 48,192,144\nM 52,208,144\nD 144,1,36160\nA 114,165\nM 52,212,148\nA 117,57\nM 52,216,152\nY 117,117,114\nM 52,220,156\nA 118,61\nM 56,200,156\nK 200,-4096\nY 118,118,114\n"
"M 56,196,152\ns_waitcnt lgkmcnt(6)\nC 65,108,118\nM 56,204,128\nC 69,107,117\nM 56,192,148\nL 104,166\nM 60,208,148\nD 148,1,36416\nL 107,58\nM 60,212,152\nD 152,1,36672\n"
"Y 107,107,104\nM 60,216,156\nD 156,1,36928\nL 108,62\nM 60,220,128\ns_waitcnt vmcnt(19) lgkmcnt(8)\nD 128,1,37184\nY 108,108,104\nM 0,224,132\ns_waitcnt vmcnt(18) lgkmcnt(7)\n"
"K 196,-3072\nA 114,166\nM 0,228,136\ns_waitcnt vmcnt(18) lgkmcnt(5)\nA 117,58\nM 0,232,140\ns_waitcnt vmcnt(17) lgkmcnt(4)\nM 0,236,144\ns_waitcnt vmcnt(16)\nY 117,117,114\n"
"M 4,252,144\ns_waitcnt vmcnt(15)\nK 204,-2048\nA 118,62\nM 4,240,132\ns_waitcnt vmcnt(15)\nD 132,1,37472\nY 118,118,114\nM 4,244,136\ns_waitcnt vmcnt(14)\nC 66,108,118\nM 4,248,140\n"
"C 70,107,117\nM 8,228,140\nG 40,8,3456\nM 8,232,144\ns_waitcnt lgkmcnt(4)\nL 104,167\nL 107,59\nM 8,236,148\nY 107,107,104\nM 8,224,136\nL 108,63\nM 12,240,136\nD 136,1,37728\n"
"Y 108,108,104\nM 12,244,140\nG 44,9,1408\nM 12,248,144\nA 114,167\nA 117,59\nM 12,252,148\nY 117,117,114\nM 16,232,148\nM 16,228,144\ns_waitcnt lgkmcnt(4)\nG 48,9,3456\n"
"M 16,236,152\nA 118,63\nY 118,118,114\nM 16,224,140\nC 67,108,118\nM 20,240,140\nD 140,1,37984\nC 71,107,117\nM 20,244,144\nG 52,10,1408\nM 20,248,148\nS 3,64,256\nM 20,252,152\n"
"L 104,168\nL 107,24\nM 24,232,152\nY 107,107,104\nM 24,228,148\ns_waitcnt lgkmcnt(5)\nK 192,-1024\nL 108,28\nM 24,236,156\nY 108,108,104\nM 24,224,144\nM 28,240,144\nD 144,2,0\n"
"A 114,168\nM 28,244,148\nK 208,3072\nA 117,24\nM 28,248,152\nS 3,68,17920\nM 28,252,156\nY 117,117,114\nM 32,232,156\nA 118,28\nM 32,228,152\ns_waitcnt lgkmcnt(6)\nK 212,0\n"
"Y 118,118,114\nM 32,236,128\nC 64,108,118\nM 32,224,148\nC 68,107,117\nM 36,240,148\nD 148,2,256\nL 104,169\nM 36,244,152\nK 216,1024\nL 107,25\nM 36,248,156\nY 107,107,104\n"
"M 36,252,128\nL 108,29\nM 40,232,128\nY 108,108,104\nM 40,228,156\ns_waitcnt lgkmcnt(6)\nK 220,2048\nv_add_u32 v15, 0xe000, v15\nM 40,236,132\nA 114,169\nA 117,25\nM 40,224,152\n"
"Y 117,117,114\nM 44,240,152\nD 152,2,512\nA 118,29\nM 44,244,156\nG 56,10,3456\nM 44,248,128\nY 118,118,114\nM 44,252,132\nC 65,108,118\nM 48,232,132\nC 69,107,117\nM 48,228,128\n"
"s_waitcnt lgkmcnt(6)\nG 60,11,1408\nM 48,236,136\nL 104,170\nL 107,26\nM 48,224,156\nY 107,107,104\nM 52,240,156\nD 156,2,768\nL 108,30\nM 52,244,128\nY 108,108,104\nM 52,248,132\n"
"A 114,170\nM 52,252,136\nA 117,26\nM 56,232,136\nK 232,-4096\nY 117,117,114\nM 56,228,132\ns_waitcnt lgkmcnt(6)\nA 118,30\nM 56,236,140\nY 118,118,114\nM 56,224,128\nM 60,240,128\n"
"D 128,2,1024\nC 66,108,118\nM 60,244,132\nD 132,2,1280\nC 70,107,117\nM 60,248,136\nD 136,2,1536\nL 104,171\nM 60,252,140\ns_waitcnt vmcnt(22) lgkmcnt(7)\nD 140,2,1792\nL 107,27\n"
"M 64,160,144\ns_waitcnt vmcnt(21) lgkmcnt(6)\nK 228,-3072\nY 107,107,104\nM 64,164,148\ns_waitcnt vmcnt(21) lgkmcnt(5)\nL 108,31\nM 64,168,152\ns_waitcnt vmcnt(20) lgkmcnt(4)\n"
"Y 108,108,104\nM 64,172,156\ns_waitcnt vmcnt(19)\nA 114,171\nM 68,188,156\ns_waitcnt vmcnt(18)\nK 236,-2048\nA 117,27\nM 68,176,144\ns_waitcnt vmcnt(18)\nD 144,2,2080\n"
"Y 117,117,114\nM 68,180,148\ns_waitcnt vmcnt(17)\nA 118,31\nM 68,184,152\nY 118,118,114\nM 72,164,152\nG 24,11,3456\nM 72,168,156\ns_waitcnt lgkmcnt(4)\nC 67,108,118\nC 71,107,117\n"
"M 72,172,128\nM 72,160,148\nM 76,176,148\nD 148,2,2336\nL 104,172\nM 76,180,152\nG 28,12,1408\nM 76,184,156\nS 3,64,512\nM 76,188,128\nL 107,32\nY 107,107,104\nM 80,168,128\n"
"L 108,36\nY 108,108,104\nM 80,164,156\ns_waitcnt lgkmcnt(5)\nK 224,-1024\nM 80,172,132\nS 3,68,18176\nM 80,160,152\nA 114,172\nA 117,32\nM 84,176,152\nY 117,117,114\nM 84,180,156\n"
"D 152,2,2592\nA 118,36\nM 84,184,128\nK 240,3072\nY 118,118,114\nM 84,188,132\nC 64,108,118\nM 88,168,132\nM 88,164,128\ns_waitcnt lgkmcnt(6)\nC 68,107,117\nM 88,172,136\nK 244,0\n"
"L 104,173\nM 88,160,156\nL 107,33\nM 92,176,156\nD 156,2,64\nY 107,107,104\nM 92,180,128\nL 108,37\nM 92,184,132\nK 248,1024\nY 108,108,104\nM 92,188,136\nA 114,173\nM 96,168,136\n"
"A 117,33\nM 96,164,132\ns_waitcnt lgkmcnt(6)\nY 117,117,114\nM 96,172,140\nK 252,2048\nv_add_u32 v15, 0x2000, v15\nM 96,160,128\nA 118,37\nY 118,118,114\nM 100,176,128\nD 128,2,320\n"
"C 65,108,118\nM 100,180,132\nC 69,107,117\nM 100,184,136\nbuffer_load_dword v192, v199, s[28:31], 0 offen offset:0\nM 100,188,140\nL 104,174\nM 104,168,140\nL 107,34\nM 104,164,136\n"
"s_waitcnt lgkmcnt(6)\nY 107,107,104\nM 104,172,144\nL 108,38\nM 104,160,132\nY 108,108,104\nM 108,176,132\nD 132,2,576\nA 114,174\nM 108,180,136\nA 117,34\nM 108,184,140\n"
"Y 117,117,114\nM 108,188,144\nA 118,38\nM 112,168,144\nY 118,118,114\nM 112,164,140\ns_waitcnt lgkmcnt(6)\nC 66,108,118\nM 112,172,148\nC 70,107,117\nM 112,160,136\nL 104,175\n"
"M 116,176,136\nD 136,2,832\nL 107,35\nM 116,180,140\nM 116,184,144\nY 107,107,104\nM 116,188,148\nL 108,39\nM 120,168,148\nK 168,-4096\nY 108,108,104\nM 120,164,144\n"
"s_waitcnt lgkmcnt(4)\nA 114,175\nM 120,172,152\nA 117,35\nM 120,160,140\nY 117,117,114\nM 124,176,140\nD 140,2,1088\nA 118,39\nM 124,180,144\nD 144,2,1344\nY 118,118,114\n"
"M 124,184,148\nD 148,2,1600\nC 67,108,118\nM 124,188,152\ns_waitcnt vmcnt(25) lgkmcnt(6)\nD 152,2,1856\nC 71,107,117\nM 64,200,156\ns_waitcnt vmcnt(24) lgkmcnt(6)\nK 164,-3072\n"
"M 64,196,128\ns_waitcnt vmcnt(24) lgkmcnt(5)\nS 3,64,768\nM 64,204,132\ns_waitcnt vmcnt(19) lgkmcnt(5)\nL 104,89\nL 106,19\nM 64,192,136\n.Lpad3:\n.if (.Lpad3 - .Lbase) != 12296\n"
".error \"pad\"\n.endif\ns_cbranch_execz .Lpad7\ns_waitcnt vmcnt(18)\nM 68,208,136\ns_waitcnt vmcnt(17)\nK 172,-2048\nY 106,106,104\nM 68,212,156\ns_waitcnt vmcnt(17)\nD 156,2,2144\n"
"L 105,21\nM 68,216,128\ns_waitcnt vmcnt(16)\nY 105,105,104\nM 68,220,132\nS 3,68,18432\nM 72,196,132\nK 160,-1024\nA 114,89\nM 72,204,136\ns_waitcnt lgkmcnt(6)\nA 116,19\n"
"M 72,192,140\nY 116,116,114\nM 72,200,128\nA 115,21\nM 76,212,128\nD 128,2,2400\nY 115,115,114\nM 76,216,132\nH 19,13,2432\nM 76,220,136\nC 64,105,115\nC 65,106,116\nM 76,208,140\n"
"H 21,14,384\nM 80,204,140\nU 6,64,0\nM 80,196,136\ns_waitcnt lgkmcnt(7)\nK 176,3072\nM 80,192,144\nU 6,65,17664\nM 80,200,132\nL 104,176\nL 107,40\nM 84,212,132\nD 132,2,2656\n"
"Y 107,107,104\nM 84,216,136\nK 180,0\nL 108,44\nM 84,220,140\nY 108,108,104\nM 84,208,144\nA 114,176\nM 88,204,144\nA 117,40\nM 88,196,140\ns_waitcnt lgkmcnt(0)\ns_barrier\n"
"Y 117,117,114\nM 88,192,148\nK 184,1024\nA 118,44\nM 88,200,136\nY 118,118,114\nM 92,212,136\nD 136,2,17664\nC 64,108,118\nM 92,216,140\nC 68,107,117\nM 92,220,144\nK 188,2048\n"
"v_add_u32 v15, 0xffff2000, v15\nM 92,208,148\nL 104,177\nL 107,41\nM 96,204,148\nM 96,196,144\nY 107,107,104\nM 96,192,152\nL 108,45\nM 96,200,140\nY 108,108,104\nM 100,212,140\n"
"D 140,2,17920\nA 114,177\nM 100,216,144\nA 117,41\nM 100,220,148\nY 117,117,114\nM 100,208,152\nA 118,45\nM 104,204,152\nY 118,118,114\nM 104,196,148\nC 65,108,118\nM 104,192,156\n"
"C 69,107,117\nM 104,200,144\nL 104,178\nM 108,212,144\nD 144,2,18176\nL 107,42\nM 108,216,148\nY 107,107,104\nM 108,220,152\nM 108,208,156\nL 108,46\nM 112,204,156\nY 108,108,104\n"
"M 112,196,152\nA 114,178\nM 112,192,128\nA 117,42\nM 112,200,148\nY 117,117,114\nM 116,212,148\nD 148,2,18432\nA 118,46\nM 116,216,152\nY 118,118,114\nM 116,220,156\nC 66,108,118\n"
"M 116,208,128\nC 70,107,117\nM 120,204,128\nK 204,-4096\nL 104,179\nM 120,196,156\nL 107,43\nM 120,192,132\nY 107,107,104\nM 120,200,152\nL 108,47\nM 124,212,152\nD 152,2,18688\n"
"Y 108,108,104\nM 124,216,156\nD 156,2,18944\nM 124,220,128\nD 128,2,19200\nA 114,179\nM 124,208,132\ns_waitcnt vmcnt(21) lgkmcnt(6)\nD 132,2,19456\nA 117,43\nM 0,232,136\n"
"s_waitcnt vmcnt(20) lgkmcnt(6)\nK 196,-3072\nY 117,117,114\nM 0,228,140\ns_waitcnt vmcnt(20) lgkmcnt(5)\nA 118,47\nM 0,236,144\ns_waitcnt vmcnt(17) lgkmcnt(4)\nY 118,118,114\n"
"M 0,224,148\ns_waitcnt vmcnt(16)\nC 67,108,118\nM 4,240,148\ns_waitcnt vmcnt(15)\nK 192,-2048\nC 71,107,117\nM 4,244,136\ns_waitcnt vmcnt(15)\nD 136,2,19744\nM 4,248,140\n"
"s_waitcnt vmcnt(14)\nS 3,64,35328\nM 4,252,144\nL 104,180\nL 107,48\nM 8,228,144\nK 200,-1024\nY 107,107,104\nM 8,236,148\ns_waitcnt lgkmcnt(5)\nL 108,52\nM 8,224,152\n"
"Y 108,108,104\nM 8,232,140\nM 12,244,140\nD 140,2,20000\nM 12,248,144\nG 32,8,2048\nM 12,252,148\nS 4,68,0\nM 12,240,152\nA 114,180\nA 117,48\nM 16,236,152\nY 117,117,114\nA 118,52\n"
"M 16,228,148\ns_waitcnt lgkmcnt(6)\nG 36,9,2048\nM 16,224,156\nY 118,118,114\nC 64,108,118\nM 16,232,144\nC 68,107,117\nM 20,244,144\nD 144,2,20256\nL 104,181\nM 20,248,148\n"
"G 40,10,2048\nM 20,252,152\nL 107,49\nY 107,107,104\nM 20,240,156\nL 108,53\nM 24,236,156\nY 108,108,104\nM 24,228,152\ns_waitcnt lgkmcnt(6)\nK 212,3072\nM 24,224,128\nA 114,181\n"
"M 24,232,148\nA 117,49\nM 28,244,148\nD 148,2,17728\nY 117,117,114\nM 28,248,152\nK 216,0\nA 118,53\nM 28,252,156\nY 118,118,114\nM 28,240,128\nC 65,108,118\nM 32,236,128\n"
"C 69,107,117\nM 32,228,156\ns_waitcnt lgkmcnt(6)\nK 220,1024\nL 104,182\nM 32,224,132\nL 107,50\nM 32,232,152\nY 107,107,104\nM 36,244,152\nD 152,2,17984\nL 108,54\nM 36,248,156\n"
"K 208,2048\nv_add_u32 v15, 0x2000, v15\nM 36,252,128\nY 108,108,104\nA 114,182\nM 36,240,132\nA 117,50\nM 40,236,132\nM 40,228,128\ns_waitcnt lgkmcnt(6)\nG 44,11,2048\nM 40,224,136\n"
"Y 117,117,114\nA 118,54\nM 40,232,156\nY 118,118,114\nM 44,244,156\nD 156,2,18240\nC 66,108,118\nM 44,248,128\nC 70,107,117\nM 44,252,132\nL 104,183\nM 44,240,136\nL 107,51\n"
"M 48,236,136\nY 107,107,104\nM 48,228,132\ns_waitcnt lgkmcnt(5)\nL 108,55\nM 48,224,140\nY 108,108,104\nM 48,232,128\nA 114,183\nM 52,244,128\nD 128,2,18496\nA 117,51\nM 52,248,132\n"
"G 48,12,2048\nM 52,252,136\nY 117,117,114\nM 52,240,140\nA 118,55\nM 56,236,140\nY 118,118,114\nM 56,228,136\ns_waitcnt lgkmcnt(4)\nH 18,13,1024\nM 56,224,144\nC 67,108,118\n"
"C 71,107,117\nM 56,232,132\nH 22,14,1024\nM 60,244,132\nD 132,2,18752\nL 104,184\nM 60,248,136\nD 136,2,19008\nL 107,56\nM 60,252,140\nD 140,2,19264\nY 107,107,104\nM 60,240,144\n"
"s_waitcnt vmcnt(24) lgkmcnt(6)\nD 144,2,19520\nL 108,60\nM 0,168,148\ns_waitcnt vmcnt(23) lgkmcnt(6)\nG 52,8,2176\nM 0,164,152\ns_waitcnt vmcnt(23) lgkmcnt(5)\nS 3,64,35584\n"
"M 0,172,156\ns_waitcnt vmcnt(22) lgkmcnt(5)\nY 108,108,104\nA 114,184\nM 0,160,128\ns_waitcnt vmcnt(19)\nA 117,56\nM 4,176,128\ns_waitcnt vmcnt(18)\nK 236,-4096\nM 4,180,148\n"
"s_waitcnt vmcnt(18)\nD 148,2,19808\nY 117,117,114\nM 4,184,152\ns_waitcnt vmcnt(17)\nS 4,68,256\nM 4,188,156\nA 118,60\nY 118,118,114\nM 8,164,156\nK 228,-3072\nC 64,108,118\n"
"M 8,172,128\ns_waitcnt lgkmcnt(6)\nC 68,107,117\nM 8,160,132\nL 104,185\nM 8,168,152\nL 107,57\nM 12,180,152\nD 152,2,20064\nY 107,107,104\nM 12,184,156\nK 224,-2048\nL 108,61\n"
"M 12,188,128\nY 108,108,104\nM 12,176,132\nA 114,185\nM 16,172,132\nA 117,57\nM 16,164,128\ns_waitcnt lgkmcnt(6)\nK 232,-1024\nY 117,117,114\nM 16,160,136\nA 118,61\nM 16,168,156\n"
"M 20,180,156\nD 156,2,20320\nY 118,118,114\nM 20,184,128\nK 244,3072\nC 65,108,118\nM 20,188,132\nC 69,107,117\nM 20,176,136\nL 104,186\nM 24,172,136\nL 107,58\nM 24,164,132\n"
"s_waitcnt lgkmcnt(6)\nK 248,0\nY 107,107,104\nM 24,160,140\nL 108,62\nM 24,168,128\nY 108,108,104\nM 28,180,128\nD 128,0,0\nA 114,186\nM 28,184,132\nK 252,1024\nA 117,58\n"
"M 28,188,136\nY 117,117,114\nM 28,176,140\nA 118,62\nM 32,172,140\nY 118,118,114\nM 32,164,136\ns_waitcnt lgkmcnt(6)\nK 240,2048\nv_add_u32 v15, 0xe000, v15\nM 32,160,144\n"
"C 66,108,118\nM 32,168,132\nC 70,107,117\nM 36,180,132\nD 132,0,256\nL 104,187\nM 36,184,136\nL 107,59\nM 36,188,140\nY 107,107,104\nM 36,176,144\nL 108,63\nM 40,172,144\n"
"Y 108,108,104\nM 40,164,140\ns_waitcnt lgkmcnt(5)\nA 114,187\nM 40,160,148\nA 117,59\nM 40,168,136\nG 56,9,2176\nM 44,180,136\nD 136,0,512\nY 117,117,114\nM 44,184,140\nA 118,63\n"
"Y 118,118,114\nM 44,188,144\nC 67,108,118\nM 44,176,148\nG 60,10,2176\nM 48,172,148\nC 71,107,117\nM 48,164,144\ns_waitcnt lgkmcnt(4)\nS 3,64,35840\nM 48,160,152\nL 104,188\n"
"M 48,168,140\nL 107,24\nM 52,180,140\nD 140,0,768\nY 107,107,104\nM 52,184,144\nL 108,28\nM 52,188,148\nY 108,108,104\nM 52,176,152\nS 4,68,512\nM 56,172,152\nK 172,-4096\n"
"A 114,188\nM 56,164,148\ns_waitcnt lgkmcnt(6)\nA 117,24\nM 56,160,156\nY 117,117,114\nM 56,168,144\nA 118,28\nM 60,180,144\nD 144,0,1024\nY 118,118,114\nM 60,184,148\nD 148,0,1280\n"
"C 64,108,118\nM 60,188,152\nD 152,0,1536\nM 60,176,156\ns_waitcnt vmcnt(26) lgkmcnt(8)\nD 156,0,1792\nC 68,107,117\nM 64,204,128\ns_waitcnt vmcnt(25) lgkmcnt(8)\nK 164,-3072\n"
"L 104,189\nM 64,196,132\ns_waitcnt vmcnt(25) lgkmcnt(7)\nL 107,25\nM 64,192,136\ns_waitcnt vmcnt(24) lgkmcnt(5)\nY 107,107,104\nM 64,200,140\ns_waitcnt vmcnt(20)\nL 108,29\n"
"M 68,212,140\ns_waitcnt vmcnt(19)\nK 160,-2048\nY 108,108,104\nM 68,216,128\ns_waitcnt vmcnt(19)\nD 128,0,2080\nA 114,189\nM 68,220,132\ns_waitcnt vmcnt(18)\nA 117,25\nM 68,208,136\n"
"Y 117,117,114\nM 72,196,136\nK 168,-1024\nA 118,29\nM 72,192,140\ns_waitcnt lgkmcnt(4)\nY 118,118,114\nM 72,200,144\nC 65,108,118\nM 72,204,132\nC 69,107,117\nM 76,216,132\n"
"D 132,0,2336\nM 76,220,136\nK 180,3072\nL 104,190\nM 76,208,140\nL 107,26\nM 76,212,144\nY 107,107,104\nM 80,192,144\nL 108,30\nM 80,196,140\ns_waitcnt lgkmcnt(4)\nK 184,0\n"
"Y 108,108,104\nM 80,200,148\nA 114,190\nM 80,204,136\nA 117,26\nM 84,216,136\nD 136,0,2592\nY 117,117,114\nM 84,220,140\nK 188,1024\nA 118,30\nM 84,208,144\nY 118,118,114\n"
"M 84,212,148\nC 66,108,118\nM 88,192,148\nC 70,107,117\nM 88,196,144\ns_waitcnt lgkmcnt(4)\nK 176,2048\nv_add_u32 v15, 0x2000, v15\nM 88,200,152\nL 104,191\nL 107,27\nM 88,204,140\n"
"M 92,216,140\nD 140,0,64\nY 107,107,104\nM 92,220,144\nL 108,31\nM 92,208,148\nY 108,108,104\nM 92,212,152\nA 114,191\nM 96,192,152\nA 117,27\nM 96,196,148\ns_waitcnt lgkmcnt(4)\n"
"G 24,11,2176\nM 96,200,156\nY 117,117,114\nA 118,31\nM 96,204,144\nY 118,118,114\nM 100,216,144\nD 144,0,320\nC 67,108,118\nM 100,220,148\nG 28,12,2176\nM 100,208,152\nC 71,107,117\n"
"M 100,212,156\nS 3,64,36096\nM 104,192,156\nL 104,90\nL 106,19\nM 104,196,152\ns_waitcnt lgkmcnt(5)\nM 104,200,128\nY 106,106,104\nM 104,204,148\nL 105,21\nM 108,216,148\n"
"D 148,0,576\nY 105,105,104\nM 108,220,152\nS 4,68,768\nM 108,208,156\nA 114,90\nM 108,212,128\nA 116,19\nM 112,192,128\nY 116,116,114\nM 112,196,156\ns_waitcnt lgkmcnt(6)\nA 115,21\n"
"M 112,200,132\nY 115,115,114\nM 112,204,152\nC 64,105,115\nM 116,216,152\nD 152,0,832\nC 65,106,116\nM 116,220,156\nU 6,64,35328\nM 116,208,128\nU 6,65,52992\nM 116,212,132\n"
"M 120,192,132\nK 192,-4096\nL 80,32\nM 120,196,128\ns_waitcnt lgkmcnt(0)\ns_barrier\nA 81,32\nM 120,200,136\nL 82,33\nM 120,204,156\nA 83,33\nM 124,216,156\nD 156,0,1088\nL 84,34\n"
"M 124,220,128\nD 128,0,1344\nA 85,34\nM 124,208,132\nD 132,0,1600\nL 86,35\nM 124,212,136\n.Lpad4:\n.if (.Lpad4 - .Lbase) != 16400\n.error \"pad\"\n.endif\ns_cbranch_execz .Lpad8\n"
"s_waitcnt vmcnt(20)\nD 136,0,1856\nA 87,35\nM 64,236,140\ns_waitcnt vmcnt(19)\nK 196,-3072\nL 104,128\nM 64,228,144\ns_waitcnt vmcnt(19)\nY 105,80,104\nM 64,224,148\n"
"s_waitcnt vmcnt(18)\nM 64,232,152\ns_waitcnt vmcnt(17)\nL 80,36\nM 68,244,152\ns_waitcnt vmcnt(16)\nK 200,-2048\nY 106,80,104\nM 68,248,140\ns_waitcnt vmcnt(16)\nD 140,0,2144\n"
"A 114,128\nM 68,252,144\ns_waitcnt vmcnt(15)\nY 115,81,114\nM 68,240,148\nA 81,36\nM 72,228,148\nK 204,-1024\nY 116,81,114\nM 72,224,152\ns_waitcnt lgkmcnt(4)\nC 64,105,115\n"
"M 72,232,156\nC 68,106,116\nM 72,236,144\nL 104,129\nM 76,248,144\nD 144,0,2400\nM 76,252,148\nK 216,3072\nY 105,82,104\nM 76,240,152\nL 82,37\nM 76,244,156\nY 106,82,104\n"
"M 80,224,156\nA 114,129\nM 80,228,152\ns_waitcnt lgkmcnt(4)\nK 220,0\nY 115,83,114\nM 80,232,128\nA 83,37\nM 80,236,148\nY 116,83,114\nM 84,248,148\nD 148,0,2656\nC 65,105,115\n"
"M 84,252,152\nK 208,1024\nC 69,106,116\nM 84,240,156\nL 104,130\nM 84,244,128\nM 88,224,128\nY 105,84,104\nM 88,228,156\ns_waitcnt lgkmcnt(4)\nK 212,2048\n"
"v_add_u32 v15, 0xffff2000, v15\nM 88,232,132\nL 84,38\nY 106,84,104\nM 88,236,152\nA 114,130\nM 92,248,152\nD 152,0,17664\nY 115,85,114\nM 92,252,156\nA 85,38\nM 92,240,128\n"
"Y 116,85,114\nM 92,244,132\nC 66,105,115\nM 96,224,132\nC 70,106,116\nM 96,228,128\ns_waitcnt lgkmcnt(4)\nM 96,232,136\nglobal_store_dwordx4 v198, v[216:219], s[44:45] sc1\n"
"M 96,236,156\nv_add_u32 v198, 0x10000, v198\nL 104,131\nM 100,248,156\nD 156,0,17920\nY 105,86,104\nM 100,252,128\nL 86,39\nY 106,86,104\nM 100,240,132\nA 114,131\nM 100,244,136\n"
"Y 115,87,114\nM 104,224,136\nA 87,39\nM 104,228,132\ns_waitcnt lgkmcnt(4)\nY 116,87,114\nM 104,232,140\nC 67,105,115\nM 104,236,128\nC 71,106,116\nM 108,248,128\nD 128,0,18176\n"
"M 108,252,132\nS 4,64,35328\nM 108,240,136\nL 104,132\nM 108,244,140\nY 105,80,104\nM 112,224,140\nL 80,40\nM 112,228,136\ns_waitcnt lgkmcnt(5)\nY 106,80,104\nM 112,232,144\n"
"S 4,68,17664\nM 112,236,132\nA 114,132\nM 116,248,132\nY 115,81,114\nM 116,252,136\nD 132,0,18432\nA 81,40\nM 116,240,140\nM 116,244,144\nY 116,81,114\nM 120,224,144\nK 224,-4096\n"
"C 64,105,115\nM 120,228,140\ns_waitcnt lgkmcnt(6)\nC 68,106,116\nM 120,232,148\nL 104,133\nM 120,236,136\nY 105,82,104\nM 124,248,136\nD 136,0,18688\nL 82,41\nM 124,252,140\n"
"D 140,0,18944\nY 106,82,104\nM 124,240,144\nD 144,0,19200\nA 114,133\nM 124,244,148\ns_waitcnt vmcnt(19) lgkmcnt(8)\nD 148,0,19456\nY 115,83,114\nM 0,172,152\n"
"s_waitcnt vmcnt(18) lgkmcnt(8)\nK 228,-3072\nA 83,41\nM 0,164,156\ns_waitcnt vmcnt(18) lgkmcnt(7)\nM 0,160,128\ns_waitcnt vmcnt(17) lgkmcnt(4)\nY 116,83,114\nM 0,168,132\n"
"s_waitcnt vmcnt(16)\nC 65,105,115\nM 4,180,132\ns_waitcnt vmcnt(15)\nK 232,-2048\nC 69,106,116\nM 4,184,152\ns_waitcnt vmcnt(15)\nD 152,0,19744\nL 104,134\nM 4,188,156\n"
"s_waitcnt vmcnt(14)\nY 105,84,104\nM 4,176,128\nL 84,42\nM 8,164,128\nK 236,-1024\nY 106,84,104\nM 8,160,132\ns_waitcnt lgkmcnt(4)\nA 114,134\nM 8,168,136\nY 115,85,114\n"
"M 8,172,156\nM 12,184,156\nD 156,0,20000\nA 85,42\nM 12,188,128\nK 248,3072\nY 116,85,114\nM 12,176,132\nC 66,105,115\nM 12,180,136\nC 70,106,116\nM 16,160,136\nL 104,135\n"
"M 16,164,132\ns_waitcnt lgkmcnt(4)\nK 252,0\nY 105,86,104\nM 16,168,140\nL 86,43\nM 16,172,128\nY 106,86,104\nM 20,184,128\nD 128,0,20256\nA 114,135\nM 20,188,132\nK 240,1024\n"
"Y 115,87,114\nM 20,176,136\nM 20,180,140\nA 87,43\nM 24,160,140\nY 116,87,114\nM 24,164,136\ns_waitcnt lgkmcnt(4)\nK 244,2048\nv_add_u32 v15, 0x2000, v15\nM 24,168,144\n"
"C 67,105,115\nC 71,106,116\nM 24,172,132\nM 28,184,132\nD 132,0,17728\nL 104,136\nM 28,188,136\nS 4,64,35584\nM 28,176,140\nY 105,80,104\nL 80,44\nM 28,180,144\nY 106,80,104\n"
"M 32,160,144\nM 32,164,140\ns_waitcnt lgkmcnt(5)\nS 4,68,17920\nM 32,168,148\nglobal_store_dwordx4 v198, v[220:223], s[44:45] sc1\nM 32,172,136\nv_add_u32 v198, 0x10000, v198\n"
"A 114,136\nM 36,184,136\nD 136,0,17984\nY 115,81,114\nM 36,188,140\nA 81,44\nY 116,81,114\nM 36,176,144\nC 64,105,115\nM 36,180,148\nC 68,106,116\nM 40,160,148\nL 104,137\n"
"M 40,164,144\ns_waitcnt lgkmcnt(6)\nY 105,82,104\nM 40,168,152\nL 82,45\nM 40,172,140\nM 44,184,140\nD 140,0,18240\nY 106,82,104\nM 44,188,144\nA 114,137\nM 44,176,148\n"
"Y 115,83,114\nM 44,180,152\nA 83,45\nM 48,160,152\nY 116,83,114\nM 48,164,148\ns_waitcnt lgkmcnt(6)\nC 65,105,115\nM 48,168,156\nC 69,106,116\nM 48,172,144\nL 104,138\nM 52,184,144\n"
"D 144,0,18496\nY 105,84,104\nM 52,188,148\nM 52,176,152\nL 84,46\nM 52,180,156\nY 106,84,104\nM 56,160,156\nK 160,-4096\nA 114,138\nM 56,164,152\ns_waitcnt lgkmcnt(6)\nY 115,85,114\n"
"M 56,168,128\nA 85,46\nM 56,172,148\nY 116,85,114\nM 60,184,148\nD 148,0,18752\nC 66,105,115\nM 60,188,152\nD 152,0,19008\nC 70,106,116\nM 60,176,156\nD 156,0,19264\nL 104,139\n"
"M 60,180,128\ns_waitcnt vmcnt(18) lgkmcnt(8)\nD 128,0,19520\nY 105,86,104\nM 0,192,132\ns_waitcnt vmcnt(17) lgkmcnt(6)\nK 164,-3072\nM 0,196,136\ns_waitcnt vmcnt(17) lgkmcnt(5)\n"
"L 86,47\nM 0,200,140\ns_waitcnt vmcnt(16) lgkmcnt(4)\nY 106,86,104\nM 0,204,144\ns_waitcnt vmcnt(15)\nA 114,139\nM 4,216,144\ns_waitcnt vmcnt(14)\nK 168,-2048\nY 115,87,114\n"
"M 4,220,132\ns_waitcnt vmcnt(14)\nD 132,0,19808\nA 87,47\nM 4,208,136\ns_waitcnt vmcnt(13)\nY 116,87,114\nM 4,212,140\nC 67,105,115\nM 8,196,140\nK 172,-1024\nC 71,106,116\n"
"M 8,200,144\ns_waitcnt lgkmcnt(4)\nS 4,64,35840\nM 8,204,148\nM 8,192,136\nL 104,140\nM 12,220,136\nD 136,0,20064\nY 105,80,104\nM 12,208,140\nK 184,3072\nL 80,48\nM 12,212,144\n"
"Y 106,80,104\nM 12,216,148\nS 4,68,18176\nM 16,200,148\nA 114,140\nM 16,196,144\ns_waitcnt lgkmcnt(6)\nK 188,0\nY 115,81,114\nM 16,204,152\nA 81,48\nM 16,192,140\nY 116,81,114\n"
"M 20,220,140\nD 140,0,20320\nC 64,105,115\nM 20,208,144\nK 176,1024\nM 20,212,148\nC 68,106,116\nM 20,216,152\nL 104,141\nM 24,200,152\nY 105,82,104\nM 24,196,148\n"
"s_waitcnt lgkmcnt(6)\nK 180,2048\nv_add_u32 v15, 0xe000, v15\nM 24,204,156\nL 82,49\nY 106,82,104\nM 24,192,144\nA 114,141\nM 28,220,144\nD 144,0,35328\nY 115,83,114\nM 28,208,148\n"
"A 83,49\nM 28,212,152\nY 116,83,114\nM 28,216,156\nM 32,200,156\nC 65,105,115\nM 32,196,152\ns_waitcnt lgkmcnt(6)\nC 69,106,116\nM 32,204,128\n"
"global_store_dwordx4 v198, v[224:227], s[44:45] sc1\nM 32,192,148\nv_add_u32 v198, 0x10000, v198\nL 104,142\nM 36,220,148\nD 148,0,35584\nY 105,84,104\nM 36,208,152\nL 84,50\n"
"Y 106,84,104\nM 36,212,156\nA 114,142\nM 36,216,128\nY 115,85,114\nM 40,200,128\nA 85,50\nM 40,196,156\ns_waitcnt lgkmcnt(6)\nY 116,85,114\nM 40,204,132\nM 40,192,152\nC 66,105,115\n"
"M 44,220,152\nD 152,0,35840\nC 70,106,116\nM 44,208,156\nL 104,143\nM 44,212,128\nY 105,86,104\nM 44,216,132\nL 86,51\nM 48,200,132\nY 106,86,104\nM 48,196,128\n"
"s_waitcnt lgkmcnt(5)\nA 114,143\nM 48,204,136\nY 115,87,114\nM 48,192,156\nA 87,51\nM 52,220,156\nD 156,0,36096\nM 52,208,128\nY 116,87,114\nM 52,212,132\nC 67,105,115\n"
"M 52,216,136\nC 71,106,116\nM 56,200,136\nK 200,-4096\nM 56,196,132\ns_waitcnt lgkmcnt(4)\nS 4,64,36096\nM 56,204,140\nL 104,20\nL 105,18\nM 56,192,128\nY 105,105,104\nM 60,220,128\n"
"D 128,0,36352\nL 106,22\nM 60,208,132\nD 132,0,36608\nY 106,106,104\nM 60,212,136\nD 136,0,36864\nM 60,216,140\ns_waitcnt vmcnt(18) lgkmcnt(7)\nD 140,0,37120\nM 64,224,144\n"
"s_waitcnt vmcnt(17) lgkmcnt(7)\nK 196,-3072\nA 114,20\nM 64,228,148\ns_waitcnt vmcnt(17) lgkmcnt(6)\nS 4,68,18432\nM 64,232,152\ns_waitcnt vmcnt(16) lgkmcnt(6)\nA 115,18\n"
"Y 115,115,114\nM 64,236,156\ns_waitcnt vmcnt(15)\nA 116,22\nM 68,248,156\ns_waitcnt vmcnt(14)\nH 18,13,1152\nM 68,252,144\ns_waitcnt vmcnt(14)\nD 144,0,37408\nY 116,116,114\n"
"M 68,240,148\ns_waitcnt vmcnt(13)\nH 22,14,1152\nM 68,244,152\nC 64,105,115\nC 65,106,116\nM 72,228,152\nG 32,8,2304\nM 72,232,156\ns_waitcnt lgkmcnt(5)\nU 7,64,17664\nM 72,236,128\n"
"U 7,65,0\nM 72,224,148\nL 80,52\nA 81,52\nM 76,252,148\nD 148,0,37664\nL 82,53\nM 76,240,152\nG 36,9,2304\nM 76,244,156\nA 83,53\nL 84,54\nM 76,248,128\nA 85,54\nM 80,232,128\n"
"L 86,55\nM 80,228,156\ns_waitcnt lgkmcnt(0)\ns_barrier\nA 87,55\nM 80,236,132\nG 40,10,2304\nM 80,224,152\nL 104,144\nY 105,80,104\nM 84,252,152\nD 152,0,37920\nM 84,240,156\n"
"L 80,56\nM 84,244,128\nK 204,-2048\nY 106,80,104\nM 84,248,132\nA 114,144\nM 88,232,132\nY 115,81,114\nM 88,228,128\nA 81,56\nM 88,236,136\nK 192,-1024\nY 116,81,114\nM 88,224,156\n"
"C 64,105,115\nM 92,252,156\nD 156,0,35392\nC 68,106,116\nM 92,240,128\nL 104,145\nM 92,244,132\nK 220,3072\nM 92,248,136\nY 105,82,104\nM 96,232,136\nL 82,57\nM 96,228,132\n"
"Y 106,82,104\nM 96,236,140\nglobal_store_dwordx4 v198, v[228:231], s[44:45] sc1\nM 96,224,128\nv_add_u32 v17, 1, v126\nA 114,145\nM 100,252,128\nD 128,0,35648\nY 115,83,114\n"
"M 100,240,132\nA 83,57\nY 116,83,114\nM 100,244,136\nK 208,0\nC 65,105,115\nM 100,248,140\nC 69,106,116\nM 104,232,140\nL 104,146\nM 104,228,136\nM 104,236,144\nK 212,1024\n"
"Y 105,84,104\nM 104,224,132\nL 84,58\nM 108,252,132\nD 132,0,35904\nY 106,84,104\nM 108,240,136\nA 114,146\nM 108,244,140\nK 216,2048\nv_add_u32 v15, 0x2000, v15\nM 108,248,144\n"
"Y 115,85,114\nA 85,58\nM 112,232,144\nY 116,85,114\nM 112,228,140\nC 66,105,115\nM 112,236,148\nG 44,11,2304\nM 112,224,136\nC 70,106,116\nM 116,252,136\nD 136,0,36160\nL 104,147\n"
"M 116,240,140\nY 105,86,104\nM 116,244,144\nG 48,12,2304\nM 116,248,148\nL 86,59\nY 106,86,104\nM 120,232,148\nA 114,147\nM 120,228,144\ns_waitcnt lgkmcnt(4)\nY 115,87,114\n"
"M 120,236,152\nK 232,-4096\nA 87,59\nM 120,224,140\n.Lpad5:\n.if (.Lpad5 - .Lbase) != 20492\n.error \"pad\"\n.endif\ns_cbranch_execz .Lpad9\nY 116,87,114\nM 124,252,140\n"
"D 140,0,36416\nC 67,105,115\nM 124,240,144\nD 144,0,36672\nC 71,106,116\nM 124,244,148\nD 148,0,36928\nM 124,248,152\ns_waitcnt vmcnt(25) lgkmcnt(6)\nD 152,0,37184\nM 64,160,156\n"
"s_waitcnt vmcnt(24) lgkmcnt(6)\nK 228,-3072\nL 104,148\nM 64,164,128\ns_waitcnt vmcnt(24) lgkmcnt(5)\nS 5,64,17664\nM 64,168,132\ns_waitcnt vmcnt(23) lgkmcnt(5)\nY 105,80,104\n"
"L 80,60\nM 64,172,136\ns_waitcnt vmcnt(22)\nY 106,80,104\nM 68,184,136\ns_waitcnt vmcnt(21)\nK 236,-2048\nM 68,188,156\ns_waitcnt vmcnt(21)\nD 156,0,37472\nA 114,148\nM 68,176,128\n"
"s_waitcnt vmcnt(20)\nS 5,68,0\nM 68,180,132\nRA 192,64\nRA 193,65\nM 72,164,132\nK 224,-1024\nRA 194,66\nM 72,168,136\ns_waitcnt lgkmcnt(6)\nRA 195,67\nY 115,81,114\nM 72,172,140\n"
"A 81,60\nY 116,81,114\nM 72,160,128\nRA 196,68\nRA 197,69\nM 76,188,128\nD 128,0,37728\nRA 198,70\nM 76,176,132\nK 252,3072\nRA 199,71\nM 76,180,136\nC 64,105,115\nC 68,106,116\n"
"M 76,184,140\nRA 200,72\nRA 201,73\nM 80,168,140\nRA 202,74\nRA 203,75\nM 80,164,136\ns_waitcnt lgkmcnt(6)\nK 240,0\nL 104,149\nM 80,172,144\nY 105,82,104\nL 82,61\nM 80,160,132\n"
"RA 204,76\nRA 205,77\nM 84,188,132\nD 132,0,37984\nRA 206,78\nM 84,176,136\nK 244,1024\nRA 207,79\nM 84,180,140\nY 106,82,104\nA 114,149\nM 84,184,144\nRA 208,80\nRA 209,81\n"
"M 88,168,144\nRA 210,82\nRA 211,83\nM 88,164,140\ns_waitcnt lgkmcnt(6)\nK 248,2048\nv_add_u32 v15, 0x12000, v15\nM 88,172,148\nY 115,83,114\nA 83,61\nM 88,160,136\nRA 212,84\n"
"RA 213,85\nM 92,188,136\nD 136,1,0\nRA 214,86\nM 92,176,140\nRA 215,87\nY 116,83,114\nM 92,180,144\nC 65,105,115\nC 69,106,116\nM 92,184,148\nRA 216,88\nRA 217,89\nM 96,168,148\n"
"RA 218,90\nRA 219,91\nM 96,164,144\ns_waitcnt lgkmcnt(6)\nL 104,150\nY 105,84,104\nM 96,172,152\nL 84,62\nY 106,84,104\nM 96,160,140\nRA 220,92\nRA 221,93\nM 100,188,140\n"
"D 140,1,256\nRA 222,94\nM 100,176,144\nRA 223,95\nA 114,150\nM 100,180,148\nY 115,85,114\nA 85,62\nM 100,184,152\nRA 224,96\nRA 225,97\nM 104,168,152\nRA 226,98\nRA 227,99\n"
"M 104,164,148\ns_waitcnt lgkmcnt(5)\nY 116,85,114\nC 66,105,115\nM 104,172,156\nC 70,106,116\nL 104,151\nM 104,160,144\nRA 228,100\nRA 229,101\nM 108,188,144\nD 144,1,512\n"
"RA 230,102\nM 108,176,148\nRA 231,103\nY 105,86,104\nM 108,180,152\nL 86,63\nY 106,86,104\nM 108,184,156\nRA 232,104\nRA 233,105\nM 112,168,156\nRA 234,106\nRA 235,107\n"
"M 112,164,152\ns_waitcnt lgkmcnt(4)\nA 114,151\nY 115,87,114\nM 112,172,128\nA 87,63\nY 116,87,114\nM 112,160,148\nRA 236,108\nRA 237,109\nM 116,188,148\nD 148,1,768\nRA 238,110\n"
"M 116,176,152\nRA 239,111\nC 67,105,115\nM 116,180,156\nC 71,106,116\nL 104,152\nM 116,184,128\nS 5,64,17920\nM 120,168,128\nK 168,-4096\nRA 240,112\nM 120,164,156\n"
"s_waitcnt lgkmcnt(5)\nRA 241,113\nRA 242,114\nM 120,172,132\nRA 243,115\nY 105,80,104\nM 120,160,152\nRA 244,116\nRA 245,117\nM 124,188,152\nD 152,1,1024\nRA 246,118\nM 124,176,156\n"
"D 156,1,1280\nRA 247,119\nM 124,180,128\nD 128,1,1536\nL 80,24\nM 124,184,132\ns_waitcnt vmcnt(24) lgkmcnt(7)\nD 132,1,1792\nRA 248,120\nM 0,200,136\ns_waitcnt vmcnt(23) lgkmcnt(7)\n"
"K 164,-3072\nRA 249,121\nM 0,196,140\ns_waitcnt vmcnt(18) lgkmcnt(6)\nRA 250,122\nRA 251,123\nM 0,204,144\ns_waitcnt vmcnt(17) lgkmcnt(5)\nY 106,80,104\nA 114,152\nM 0,192,148\n"
"s_waitcnt vmcnt(16)\nS 5,68,256\nM 4,220,148\ns_waitcnt vmcnt(14)\nK 172,-2048\nRA 252,124\nM 4,208,136\ns_waitcnt vmcnt(14)\nRA 253,125\nRA 254,126\nM 4,212,140\n"
"s_waitcnt vmcnt(13)\nD 136,1,2080\nRA 255,127\nM 4,216,144\nY 115,81,114\nA 81,24\nM 8,196,144\nK 160,-1024\nY 116,81,114\nM 8,204,148\ns_waitcnt lgkmcnt(5)\nC 64,105,115\n"
"C 68,106,116\nM 8,192,152\nL 104,153\nY 105,82,104\nM 8,200,140\nL 82,25\nY 106,82,104\nM 12,208,140\nD 140,1,2336\nA 114,153\nM 12,212,144\nK 188,3072\nY 115,83,114\nM 12,216,148\n"
"A 83,25\nY 116,83,114\nM 12,220,152\nC 65,105,115\nC 69,106,116\nM 16,204,152\nL 104,154\nY 105,84,104\nM 16,196,148\ns_waitcnt lgkmcnt(5)\nK 176,0\nL 84,26\nM 16,192,156\n"
"Y 106,84,104\nA 114,154\nM 16,200,144\nY 115,85,114\nA 85,26\nM 20,208,144\nD 144,1,2592\nY 116,85,114\nM 20,212,148\nK 180,1024\nC 66,105,115\nM 20,216,152\nC 70,106,116\n"
"L 104,155\nM 20,220,156\nY 105,86,104\nL 86,27\nM 24,204,156\nY 106,86,104\nA 114,155\nM 24,196,152\ns_waitcnt lgkmcnt(5)\nK 184,2048\nv_add_u32 v15, 0x2000, v15\nM 24,192,128\n"
"Y 115,87,114\nA 87,27\nM 24,200,148\nY 116,87,114\nC 67,105,115\nM 28,208,148\nD 148,1,64\nC 71,106,116\nM 28,212,152\nS 5,64,18176\nM 28,216,156\nL 104,156\nY 105,80,104\n"
"M 28,220,128\nL 80,28\nM 32,204,128\nY 106,80,104\nM 32,196,156\ns_waitcnt lgkmcnt(6)\nS 5,68,512\nM 32,192,132\nA 114,156\nM 32,200,152\nY 115,81,114\nM 36,208,152\nD 152,1,320\n"
"A 81,28\nM 36,212,156\nY 116,81,114\nM 36,216,128\nC 64,105,115\nM 36,220,132\nM 40,204,132\nC 68,106,116\nM 40,196,128\ns_waitcnt lgkmcnt(6)\nL 104,157\nM 40,192,136\nY 105,82,104\n"
"M 40,200,156\nL 82,29\nM 44,208,156\nD 156,1,576\nY 106,82,104\nM 44,212,128\nA 114,157\nM 44,216,132\nY 115,83,114\nM 44,220,136\nA 83,29\nM 48,204,136\nY 116,83,114\nM 48,196,132\n"
"s_waitcnt lgkmcnt(6)\nM 48,192,140\nC 65,105,115\nM 48,200,128\nC 69,106,116\nM 52,208,128\nD 128,1,832\nL 104,158\nM 52,212,132\nY 105,84,104\nM 52,216,136\nL 84,30\nM 52,220,140\n"
"Y 106,84,104\nM 56,204,140\nK 204,-4096\nA 114,158\nM 56,196,136\ns_waitcnt lgkmcnt(6)\nY 115,85,114\nM 56,192,144\nA 85,30\nM 56,200,132\nY 116,85,114\nM 60,208,132\nD 132,1,1088\n"
"M 60,212,136\nD 136,1,1344\nC 66,105,115\nM 60,216,140\nD 140,1,1600\nC 70,106,116\nM 60,220,144\ns_waitcnt vmcnt(16) lgkmcnt(8)\nD 144,1,1856\nL 104,159\nM 0,232,148\n"
"s_waitcnt vmcnt(15) lgkmcnt(6)\nK 196,-3072\nY 105,86,104\nM 0,228,152\ns_waitcnt vmcnt(15) lgkmcnt(5)\nL 86,31\nM 0,236,156\ns_waitcnt vmcnt(14) lgkmcnt(4)\nY 106,86,104\n"
"M 0,224,128\ns_waitcnt vmcnt(13)\nA 114,159\nM 4,252,128\ns_waitcnt vmcnt(12)\nK 192,-2048\nY 115,87,114\nM 4,240,148\ns_waitcnt vmcnt(12)\nD 148,1,2144\nA 87,31\nM 4,244,152\n"
"s_waitcnt vmcnt(11)\nM 4,248,156\nRA 113,0\nY 192,192,113\nM 8,228,156\nK 200,-1024\nRA 113,1\nM 8,236,128\ns_waitcnt lgkmcnt(4)\nY 193,193,113\nRA 113,2\nM 8,224,132\n"
"Y 194,194,113\nRA 113,3\nM 8,232,152\nY 195,195,113\nRA 113,4\nM 12,240,152\nD 152,1,2400\nY 196,196,113\nM 12,244,156\nK 208,3072\nRA 113,5\nM 12,248,128\nY 197,197,113\nRA 113,6\n"
"M 12,252,132\nY 198,198,113\nRA 113,7\nM 16,236,132\nY 199,199,113\nRA 113,8\nM 16,228,128\ns_waitcnt lgkmcnt(4)\nK 212,0\nY 200,200,113\nM 16,224,136\nRA 113,9\nY 201,201,113\n"
"M 16,232,156\nRA 113,10\nY 202,202,113\nM 20,240,156\nD 156,1,2656\nRA 113,11\nM 20,244,128\nK 216,1024\nY 203,203,113\nM 20,248,132\nRA 113,12\nY 204,204,113\nM 20,252,136\n"
"RA 113,13\nY 205,205,113\nM 24,236,136\nRA 113,14\nY 206,206,113\nM 24,228,132\ns_waitcnt lgkmcnt(4)\nK 220,2048\nv_add_u32 v15, 0xfffee000, v15\nM 24,224,140\nRA 113,15\n"
"Y 207,207,113\nM 24,232,128\nRA 113,16\nY 208,208,113\nM 28,240,128\nD 128,1,17664\nRA 113,17\nM 28,244,132\nY 209,209,113\nRA 113,18\nM 28,248,136\nY 210,210,113\nRA 113,19\n"
"M 28,252,140\nY 211,211,113\nRA 113,20\nM 32,236,140\nY 212,212,113\nRA 113,21\nM 32,228,136\ns_waitcnt lgkmcnt(4)\nY 213,213,113\nRA 113,22\nM 32,224,144\nY 214,214,113\nRA 113,23\n"
"M 32,232,132\nY 215,215,113\nRA 113,24\nM 36,240,132\nD 132,1,17920\nY 216,216,113\nM 36,244,136\nRA 113,25\nY 217,217,113\nM 36,248,140\nRA 113,26\nY 218,218,113\nM 36,252,144\n"
"RA 113,27\nY 219,219,113\nM 40,236,144\nRA 113,28\nY 220,220,113\nM 40,228,140\ns_waitcnt lgkmcnt(4)\nRA 113,29\nY 221,221,113\nM 40,224,148\nRA 113,30\nY 222,222,113\nM 40,232,136\n"
"RA 113,31\nY 223,223,113\nM 44,240,136\nD 136,1,18176\nRA 113,32\nM 44,244,140\nY 224,224,113\nRA 113,33\nM 44,248,144\nY 225,225,113\nRA 113,34\nM 44,252,148\nY 226,226,113\n"
"RA 113,35\nM 48,236,148\nY 227,227,113\nRA 113,36\nM 48,228,144\ns_waitcnt lgkmcnt(4)\nY 228,228,113\nRA 113,37\nM 48,224,152\nY 229,229,113\nRA 113,38\nM 48,232,140\nY 230,230,113\n"
"RA 113,39\nM 52,240,140\nD 140,1,18432\nY 231,231,113\nM 52,244,144\nRA 113,40\nY 232,232,113\nM 52,248,148\nRA 113,41\nY 233,233,113\nM 52,252,152\nRA 113,42\nY 234,234,113\n"
"M 56,236,152\nK 236,-4096\nRA 113,43\nM 56,228,148\ns_waitcnt lgkmcnt(4)\nY 235,235,113\nRA 113,44\nM 56,224,156\nY 236,236,113\nRA 113,45\nM 56,232,144\nY 237,237,113\nRA 113,46\n"
"M 60,240,144\nD 144,1,18688\nY 238,238,113\nM 60,244,148\nD 148,1,18944\nRA 113,47\nM 60,248,152\nD 152,1,19200\nY 239,239,113\nM 60,252,156\ns_waitcnt vmcnt(16) lgkmcnt(6)\n"
"D 156,1,19456\nRA 113,48\nM 64,168,128\ns_waitcnt vmcnt(15) lgkmcnt(6)\nK 228,-3072\nY 240,240,113\nM 64,164,132\ns_waitcnt vmcnt(15) lgkmcnt(5)\nRA 113,49\nY 241,241,113\n"
"M 64,172,136\ns_waitcnt vmcnt(14) lgkmcnt(4)\nRA 113,50\nY 242,242,113\nM 64,160,140\ns_waitcnt vmcnt(13)\nRA 113,51\nY 243,243,113\nM 68,188,140\n.Lpad6:\n"
".if (.Lpad6 - .Lbase) != 24584\n.error \"pad\"\n.endif\ns_cbranch_execz .Lpad10\ns_waitcnt vmcnt(12)\nK 224,-2048\nRA 113,52\nM 68,176,128\ns_waitcnt vmcnt(12)\nD 128,1,19744\n"
"Y 244,244,113\nM 68,180,132\ns_waitcnt vmcnt(11)\nRA 113,53\nY 245,245,113\nM 68,184,136\nRA 113,54\nY 246,246,113\nM 72,164,136\nK 232,-1024\nRA 113,55\nM 72,172,140\n"
"s_waitcnt lgkmcnt(4)\nY 247,247,113\nRA 113,56\nM 72,160,144\nY 248,248,113\nRA 113,57\nM 72,168,132\nY 249,249,113\nRA 113,58\nM 76,176,132\nD 132,1,20000\nY 250,250,113\n"
"M 76,180,136\nK 240,3072\nRA 113,59\nM 76,184,140\nY 251,251,113\nRA 113,60\nM 76,188,144\nY 252,252,113\nRA 113,61\nM 80,172,144\nY 253,253,113\nRA 113,62\nM 80,164,140\n"
"s_waitcnt lgkmcnt(4)\nK 244,0\nY 254,254,113\nM 80,160,148\nRA 113,63\nY 255,255,113\nM 80,168,136\nY 116,87,114\nC 67,105,115\nM 84,176,136\nD 136,1,20256\nC 71,106,116\n"
"M 84,180,140\nK 248,1024\nL 104,88\nM 84,184,144\nS 5,64,18432\nM 84,188,148\nL 105,18\nY 105,105,104\nM 88,172,148\nL 106,22\nY 106,106,104\nM 88,164,144\ns_waitcnt lgkmcnt(5)\n"
"K 252,2048\nv_add_u32 v15, 0x2000, v15\nM 88,160,152\nS 5,68,768\nM 88,168,140\nA 114,88\nA 115,18\nM 92,176,140\nY 115,115,114\nA 116,22\nM 92,180,144\nD 140,1,17728\n"
"Y 116,116,114\nM 92,184,148\nH 18,13,1280\nM 92,188,152\nC 64,105,115\nC 65,106,116\nM 96,172,152\nH 22,14,1280\nM 96,164,148\ns_waitcnt lgkmcnt(6)\nU 7,64,52992\nM 96,160,156\n"
"G 52,8,2432\nM 96,168,144\nU 7,65,35328\nM 100,176,144\nD 144,1,17984\nL 80,32\nM 100,180,148\nA 81,32\nL 82,33\nM 100,184,152\nG 56,9,2432\nM 100,188,156\nA 83,33\nL 84,34\n"
"M 104,172,156\nA 85,34\nL 86,35\nM 104,164,152\ns_waitcnt lgkmcnt(0)\ns_barrier\nA 87,35\nM 104,160,128\nG 60,10,2432\nM 104,168,148\nL 104,160\nY 105,80,104\nM 108,176,148\n"
"D 148,1,18240\nL 80,36\nM 108,180,152\nY 106,80,104\nA 114,160\nM 108,184,156\nG 24,11,2432\nM 108,188,128\nY 115,81,114\nA 81,36\nM 112,172,128\nY 116,81,114\nC 64,105,115\n"
"M 112,164,156\nC 68,106,116\nL 104,161\nM 112,160,132\nG 28,12,2432\nM 112,168,152\nY 105,82,104\nL 82,37\nM 116,176,152\nD 152,1,18496\nY 106,82,104\nM 116,180,156\nA 114,161\n"
"Y 115,83,114\nM 116,184,128\nA 83,37\nY 116,83,114\nM 116,188,132\nC 65,105,115\nC 69,106,116\nM 120,172,132\nK 172,-4096\nL 104,162\nM 120,164,128\nY 105,84,104\nL 84,38\n"
"M 120,160,136\nY 106,84,104\nA 114,162\nM 120,168,156\nY 115,85,114\nA 85,38\nM 124,176,156\nD 156,1,18752\nY 116,85,114\nM 124,180,128\nD 128,1,19008\nC 66,105,115\nM 124,184,132\n"
"D 132,1,19264\nC 70,106,116\nM 124,188,136\ns_waitcnt vmcnt(23)\nD 136,1,19520\nL 104,163\nM 64,204,140\ns_waitcnt vmcnt(22)\nK 164,-3072\nY 105,86,104\nM 64,196,144\n"
"s_waitcnt vmcnt(22) lgkmcnt(5)\nL 86,39\nY 106,86,104\nM 64,192,148\ns_waitcnt vmcnt(21) lgkmcnt(4)\nA 114,163\nY 115,87,114\nM 64,200,152\ns_waitcnt vmcnt(20)\nA 87,39\n"
"Y 116,87,114\nM 68,208,152\ns_waitcnt vmcnt(19)\nK 160,-2048\nC 67,105,115\nM 68,212,140\ns_waitcnt vmcnt(19)\nD 140,1,19808\nC 71,106,116\nM 68,216,144\ns_waitcnt vmcnt(18)\n"
"S 3,64,17664\nM 68,220,148\nL 104,164\nY 105,80,104\nM 72,196,148\nK 168,-1024\nL 80,40\nM 72,192,152\ns_waitcnt lgkmcnt(5)\nY 106,80,104\nA 114,164\nM 72,200,156\nS 3,68,0\n"
"M 72,204,144\nY 115,81,114\nA 81,40\nM 76,212,144\nK 176,3072\nY 116,81,114\nM 76,216,148\nD 144,1,20064\nC 64,105,115\nM 76,220,152\nC 68,106,116\nL 104,165\nM 76,208,156\n"
"Y 105,82,104\nL 82,41\nM 80,192,156\nK 180,0\nY 106,82,104\nM 80,196,152\ns_waitcnt lgkmcnt(6)\nA 114,165\nY 115,83,114\nM 80,200,128\nA 83,41\nY 116,83,114\nM 80,204,148\n"
"C 65,105,115\nC 69,106,116\nM 84,212,148\nD 148,1,20320\nL 104,166\nM 84,216,152\nK 184,1024\nY 105,84,104\nM 84,220,156\nL 84,42\nY 106,84,104\nM 84,208,128\nA 114,166\n"
"Y 115,85,114\nM 88,192,128\nA 85,42\nY 116,85,114\nM 88,196,156\ns_waitcnt lgkmcnt(6)\nK 188,2048\nv_add_u32 v15, 0x12000, v15\nM 88,200,132\nC 66,105,115\nC 70,106,116\n"
"M 88,204,152\nL 104,167\nY 105,86,104\nM 92,212,152\nD 152,1,35328\nL 86,43\nM 92,216,156\nY 106,86,104\nA 114,167\nM 92,220,128\nY 115,87,114\nA 87,43\nM 92,208,132\nY 116,87,114\n"
"C 67,105,115\nM 96,192,132\nC 71,106,116\nL 104,168\nM 96,196,128\ns_waitcnt lgkmcnt(6)\nS 3,64,17920\nM 96,200,136\nY 105,80,104\nL 80,44\nM 96,204,156\nY 106,80,104\nA 114,168\n"
"M 100,212,156\nD 156,1,35584\nY 115,81,114\nM 100,216,128\nS 3,68,256\nM 100,220,132\nA 81,44\nY 116,81,114\nM 100,208,136\nC 64,105,115\nC 68,106,116\nM 104,192,136\nL 104,169\n"
"Y 105,82,104\nM 104,196,132\ns_waitcnt lgkmcnt(8)\nL 82,45\nY 106,82,104\nM 104,200,140\nA 114,169\nY 115,83,114\nM 104,204,128\nA 83,45\nY 116,83,114\nM 108,212,128\nD 128,1,35840\n"
"C 65,105,115\nM 108,216,132\nC 69,106,116\nL 104,170\nM 108,220,136\nY 105,84,104\nL 84,46\nM 108,208,140\nY 106,84,104\nA 114,170\nM 112,192,140\nY 115,85,114\nA 85,46\n"
"M 112,196,136\ns_waitcnt lgkmcnt(6)\nY 116,85,114\nC 66,105,115\nM 112,200,144\nC 70,106,116\nL 104,171\nM 112,204,132\nY 105,86,104\nL 86,47\nM 116,212,132\nD 132,1,36096\n"
"Y 106,86,104\nM 116,216,136\nA 114,171\nY 115,87,114\nM 116,220,140\nA 87,47\nY 116,87,114\nM 116,208,144\nC 67,105,115\nC 71,106,116\nM 120,192,144\nK 192,-4096\nL 104,172\n"
"M 120,196,140\ns_waitcnt lgkmcnt(6)\nS 3,64,18176\nM 120,200,148\nY 105,80,104\nL 80,48\nM 120,204,136\nY 106,80,104\nA 114,172\nM 124,212,136\nD 136,1,36352\nY 115,81,114\n"
"M 124,216,140\nD 140,1,36608\nA 81,48\nM 124,220,144\nD 144,1,36864\nY 116,81,114\nM 124,208,148\ns_waitcnt vmcnt(23) lgkmcnt(9)\nD 148,1,37120\nC 64,105,115\nM 0,236,152\n"
"s_waitcnt vmcnt(22) lgkmcnt(8)\nK 196,-3072\nM 0,228,156\ns_waitcnt vmcnt(22) lgkmcnt(6)\nS 3,68,512\nM 0,224,128\ns_waitcnt vmcnt(21) lgkmcnt(6)\nC 68,106,116\nL 104,173\n"
"M 0,232,132\ns_waitcnt vmcnt(20)\nY 105,82,104\nL 82,49\nM 4,240,132\ns_waitcnt vmcnt(19)\nK 200,-2048\nY 106,82,104\nM 4,244,152\ns_waitcnt vmcnt(19)\nD 152,1,37408\nA 114,173\n"
"M 4,248,156\ns_waitcnt vmcnt(18)\nY 115,83,114\nA 83,49\nM 4,252,128\nY 116,83,114\nC 65,105,115\nM 8,228,128\nK 204,-1024\nC 69,106,116\nM 8,224,132\ns_waitcnt lgkmcnt(5)\n"
"L 104,174\nY 105,84,104\nM 8,232,136\nL 84,50\nY 106,84,104\nM 8,236,156\nA 114,174\nY 115,85,114\nM 12,244,156\nD 156,1,37664\nA 85,50\nM 12,248,128\nK 212,3072\nY 116,85,114\n"
"M 12,252,132\nC 66,105,115\nC 70,106,116\nM 12,240,136\nL 104,175\nY 105,86,104\nM 16,224,136\nL 86,51\nY 106,86,104\nM 16,228,132\ns_waitcnt lgkmcnt(5)\nK 216,0\nA 114,175\n"
"M 16,232,140\nY 115,87,114\nA 87,51\nM 16,236,128\nY 116,87,114\nC 67,105,115\nM 20,244,128\nD 128,1,37920\nC 71,106,116\nM 20,248,132\nK 220,1024\nL 104,89\nM 20,252,136\n"
"S 3,64,18432\nM 20,240,140\ns_waitcnt vmcnt(21)\nL 105,18\nY 105,105,104\nM 24,224,140\ns_waitcnt vmcnt(20)\nL 106,22\nY 106,106,104\nM 24,228,136\ns_waitcnt lgkmcnt(6)\nK 208,2048\n"
"v_add_u32 v15, 0x2000, v15\nM 24,232,144\nS 3,68,768\nM 24,236,132\nA 114,89\nA 115,18\nM 28,244,132\nY 115,115,114\nA 116,22\nM 28,248,136\nD 132,1,35392\nY 116,116,114\n"
"M 28,252,140\nH 18,13,1408\nM 28,240,144\nC 64,105,115\nC 65,106,116\nM 32,224,144\nH 22,14,1408\nM 32,228,140\ns_waitcnt lgkmcnt(7)\nU 6,64,17664\nM 32,232,148\nG 32,8,1024\n"
"M 32,236,136\nU 6,65,0\nM 36,244,136\ns_waitcnt vmcnt(23)\nD 136,1,35648\nL 80,52\nM 36,248,140\nA 81,52\nL 82,53\nM 36,252,144\nG 36,8,2048\nM 36,240,148\nA 83,53\nL 84,54\n"
"M 40,224,148\nA 85,54\nL 86,55\nM 40,228,144\ns_waitcnt lgkmcnt(8)\nA 87,55\nL 104,176\nM 40,232,152\nG 40,8,3072\nM 40,236,140\ns_waitcnt vmcnt(24)\nY 105,80,104\nL 80,56\n"
"M 44,244,140\nD 140,1,35904\nY 106,80,104\nM 44,248,144\nA 114,176\nY 115,81,114\nM 44,252,148\nG 44,9,0\nM 44,240,152\nA 81,56\nY 116,81,114\nM 48,224,152\nC 64,105,115\n"
"C 68,106,116\nM 48,228,148\ns_waitcnt lgkmcnt(8)\nL 104,177\nY 105,82,104\nM 48,232,156\nG 48,9,1024\nM 48,236,144\nL 82,57\nY 106,82,104\nM 52,244,144\nD 144,1,36160\nA 114,177\n"
"M 52,248,148\nY 115,83,114\nA 83,57\nM 52,252,152\nG 52,9,2048\nM 52,240,156\nY 116,83,114\nC 65,105,115\nM 56,224,156\nC 69,106,116\nL 104,178\nM 56,228,152\ns_waitcnt lgkmcnt(8)\n"
"Y 105,84,104\nL 84,58\nM 56,232,128\nK 224,-4096\nY 106,84,104\nM 56,236,148\nA 114,178\nY 115,85,114\nM 60,244,148\nD 148,1,36416\nA 85,58\nM 60,248,152\nD 152,1,36672\n"
"Y 116,85,114\nM 60,252,156\nD 156,1,36928\nC 66,105,115\nM 60,240,128\ns_waitcnt vmcnt(24) lgkmcnt(8)\nD 128,1,37184\nC 70,106,116\nM 0,172,132\ns_waitcnt vmcnt(23) lgkmcnt(6)\n"
"K 228,-3072\nL 104,179\nM 0,164,136\ns_waitcnt vmcnt(23) lgkmcnt(5)\nY 105,86,104\nL 86,59\nM 0,160,140\ns_waitcnt vmcnt(22) lgkmcnt(4)\nY 106,86,104\nA 114,179\nM 0,168,144\n"
"s_waitcnt vmcnt(21)\nY 115,87,114\nA 87,59\nM 4,176,144\ns_waitcnt vmcnt(20)\nG 56,9,3072\nM 4,180,132\ns_waitcnt vmcnt(20)\nD 132,1,37472\nY 116,87,114\nM 4,184,136\n"
"s_waitcnt vmcnt(19)\nC 67,105,115\nC 71,106,116\nM 4,188,140\nS 3,64,35328\nM 8,164,140\nK 232,-2048\nL 104,180\nM 8,160,144\ns_waitcnt lgkmcnt(5)\nY 105,80,104\nL 80,60\n"
"M 8,168,148\nY 106,80,104\nA 114,180\nM 8,172,136\nY 115,81,114\nA 81,60\nM 12,180,136\nD 136,1,37728\nY 116,81,114\nM 12,184,140\nK 236,-1024\nC 64,105,115\nM 12,188,144\nS 4,68,0\n"
"M 12,176,148\nC 68,106,116\nL 104,181\nM 16,160,148\nY 105,82,104\nL 82,61\nM 16,164,144\ns_waitcnt lgkmcnt(6)\nK 244,3072\nY 106,82,104\nM 16,168,152\nA 114,181\nY 115,83,114\n"
"M 16,172,140\nA 83,61\nY 116,83,114\nM 20,180,140\nD 140,1,37984\nC 65,105,115\nM 20,184,144\nK 248,0\nC 69,106,116\nM 20,188,148\nL 104,182\nY 105,84,104\nM 20,176,152\n.Lpad7:\n"
".if (.Lpad7 - .Lbase) != 28684\n.error \"pad\"\n.endif\ns_cbranch_execz .Lpad11\nL 84,62\nY 106,84,104\nM 24,160,152\nA 114,182\nY 115,85,114\nM 24,164,148\ns_waitcnt lgkmcnt(6)\n"
"K 252,1024\nA 85,62\nM 24,168,156\nY 116,85,114\nC 66,105,115\nM 24,172,144\nC 70,106,116\nL 104,183\nM 28,180,144\nD 144,2,0\nY 105,86,104\nM 28,184,148\nK 240,2048\n"
"v_add_u32 v15, 0xfffee000, v15\nM 28,188,152\nL 86,63\nY 106,86,104\nM 28,176,156\nA 114,183\nM 32,160,156\nY 115,87,114\nM 32,164,152\ns_waitcnt lgkmcnt(6)\nA 87,63\nM 32,168,128\n"
"G 60,10,0\nM 32,172,148\nY 116,87,114\nC 67,105,115\nM 36,180,148\nD 148,2,256\nC 71,106,116\nM 36,184,152\nS 3,64,35584\nM 36,188,156\nL 104,184\nM 36,176,128\nY 105,80,104\n"
"M 40,160,128\nL 80,24\nM 40,164,156\ns_waitcnt lgkmcnt(7)\nY 106,80,104\nM 40,168,132\nS 4,68,256\nM 40,172,152\nA 114,184\nY 115,81,114\nM 44,180,152\nA 81,24\nM 44,184,156\n"
"D 152,2,512\nY 116,81,114\nM 44,188,128\nC 64,105,115\nM 44,176,132\nC 68,106,116\nM 48,160,132\nL 104,185\nM 48,164,128\ns_waitcnt lgkmcnt(7)\nY 105,82,104\nM 48,168,136\nL 82,25\n"
"M 48,172,156\nY 106,82,104\nM 52,180,156\nD 156,2,768\nA 114,185\nM 52,184,128\nY 115,83,114\nM 52,188,132\nA 83,25\nM 52,176,136\nY 116,83,114\nM 56,160,136\nK 160,-4096\n"
"C 65,105,115\nM 56,164,132\ns_waitcnt lgkmcnt(6)\nC 69,106,116\nM 56,168,140\nL 104,186\nM 56,172,128\nY 105,84,104\nM 60,180,128\nD 128,2,1024\nL 84,26\nM 60,184,132\nD 132,2,1280\n"
"Y 106,84,104\nM 60,188,136\nD 136,2,1536\nA 114,186\nM 60,176,140\ns_waitcnt vmcnt(26) lgkmcnt(8)\nD 140,2,1792\nY 115,85,114\nM 64,192,144\ns_waitcnt vmcnt(25) lgkmcnt(8)\n"
"K 164,-3072\nA 85,26\nM 64,196,148\ns_waitcnt vmcnt(25) lgkmcnt(5)\nY 116,85,114\nC 66,105,115\nM 64,200,152\ns_waitcnt vmcnt(24) lgkmcnt(4)\nC 70,106,116\nM 64,204,156\n"
"s_waitcnt vmcnt(23)\nL 104,187\nM 68,212,156\ns_waitcnt vmcnt(22)\nK 168,-2048\nY 105,86,104\nM 68,216,144\ns_waitcnt vmcnt(22)\nD 144,2,2080\nL 86,27\nM 68,220,148\n"
"s_waitcnt vmcnt(21)\nY 106,86,104\nM 68,208,152\nA 114,187\nM 72,196,152\nK 172,-1024\nY 115,87,114\nM 72,200,156\ns_waitcnt lgkmcnt(4)\nA 87,27\nM 72,204,128\nY 116,87,114\n"
"M 72,192,148\nC 67,105,115\nM 76,216,148\nD 148,2,2336\nC 71,106,116\nM 76,220,152\nG 24,10,1024\nM 76,208,156\nS 3,64,35840\nM 76,212,128\nL 104,188\nY 105,80,104\nM 80,200,128\n"
"L 80,28\nY 106,80,104\nM 80,196,156\ns_waitcnt lgkmcnt(5)\nK 180,3072\nM 80,204,132\nS 4,68,512\nM 80,192,152\nA 114,188\nY 115,81,114\nM 84,216,152\nA 81,28\nM 84,220,156\n"
"D 152,2,2592\nY 116,81,114\nM 84,208,128\nK 184,0\nC 64,105,115\nM 84,212,132\nC 68,106,116\nM 88,200,132\nL 104,189\nM 88,196,128\ns_waitcnt lgkmcnt(6)\nY 105,82,104\nM 88,204,136\n"
"K 188,1024\nL 82,29\nM 88,192,156\nY 106,82,104\nM 92,216,156\nD 156,2,64\nA 114,189\nM 92,220,128\nY 115,83,114\nM 92,208,132\nK 176,2048\nv_add_u32 v15, 0x2000, v15\nM 92,212,136\n"
"A 83,29\nY 116,83,114\nM 96,200,136\nC 65,105,115\nC 69,106,116\nM 96,196,132\ns_waitcnt lgkmcnt(6)\nL 104,190\nM 96,204,140\nY 105,84,104\nM 96,192,128\nL 84,30\nM 100,216,128\n"
"D 128,2,320\nY 106,84,104\nM 100,220,132\nA 114,190\nM 100,208,136\nY 115,85,114\nM 100,212,140\nA 85,30\nM 104,200,140\nY 116,85,114\nM 104,196,136\ns_waitcnt lgkmcnt(6)\n"
"C 66,105,115\nM 104,204,144\nC 70,106,116\nM 104,192,132\nL 104,191\nM 108,216,132\nD 132,2,576\nY 105,86,104\nM 108,220,136\nL 86,31\nM 108,208,140\nY 106,86,104\nM 108,212,144\n"
"A 114,191\nM 112,200,144\nY 115,87,114\nM 112,196,140\ns_waitcnt lgkmcnt(6)\nA 87,31\nM 112,204,148\nG 28,10,2048\nM 112,192,136\nY 116,87,114\nC 67,105,115\nM 116,216,136\n"
"D 136,2,832\nC 71,106,116\nM 116,220,140\nS 3,64,36096\nM 116,208,144\ns_waitcnt vmcnt(27)\nL 104,90\nL 105,18\nM 116,212,148\nY 105,105,104\nM 120,200,148\ns_waitcnt vmcnt(26)\n"
"K 200,-4096\nL 106,22\nM 120,196,144\ns_waitcnt lgkmcnt(5)\nY 106,106,104\nM 120,204,152\nS 4,68,768\nM 120,192,140\nA 114,90\nM 124,216,140\nK 196,-3072\nA 115,18\nM 124,220,144\n"
"D 140,2,1088\nY 115,115,114\nM 124,208,148\nD 144,2,1344\nA 116,22\nM 124,212,152\ns_waitcnt vmcnt(21) lgkmcnt(7)\nD 148,2,1600\nY 116,116,114\nM 64,224,156\n"
"s_waitcnt vmcnt(20) lgkmcnt(7)\nD 152,2,1856\nC 64,105,115\nM 64,228,128\ns_waitcnt vmcnt(18) lgkmcnt(7)\nK 204,-2048\nC 65,106,116\nM 64,232,132\ns_waitcnt vmcnt(18) lgkmcnt(6)\n"
"U 6,64,35328\nM 64,236,136\ns_waitcnt vmcnt(17)\nU 6,65,52992\nM 68,244,136\ns_waitcnt vmcnt(16)\nL 104,32\nL 80,36\nM 68,248,156\ns_waitcnt vmcnt(15)\nD 156,2,2144\nL 88,40\n"
"M 68,252,128\ns_waitcnt vmcnt(14)\nK 192,-1024\nY 96,104,88\nM 68,240,132\nA 105,32\nA 81,36\nM 72,228,132\nA 89,40\nY 97,105,89\nM 72,232,136\ns_waitcnt lgkmcnt(6)\nL 104,33\n"
"L 82,37\nM 72,236,140\nK 216,3072\nL 90,41\nM 72,224,128\nY 98,104,90\nA 105,33\nM 76,248,128\nD 128,2,2400\nA 83,37\nM 76,252,132\nA 91,41\nY 99,105,91\nM 76,240,136\nK 220,0\n"
"L 104,34\nM 76,244,140\nL 84,38\nL 92,42\nM 80,232,140\nY 100,104,92\nA 105,34\nM 80,228,136\ns_waitcnt lgkmcnt(6)\nA 85,38\nA 93,42\nM 80,236,144\nK 208,1024\nY 101,105,93\n"
"M 80,224,132\nL 104,35\nL 86,39\nM 84,248,132\nD 132,2,2656\nL 94,43\nM 84,252,136\nY 102,104,94\nA 105,35\nM 84,240,140\nG 32,10,3072\nM 84,244,144\nA 87,39\nA 95,43\nM 88,232,144\n"
"Y 103,105,95\nL 104,44\nM 88,228,140\ns_waitcnt lgkmcnt(6)\nL 105,48\nY 106,104,80\nM 88,236,148\nK 212,2048\nv_add_u32 v15, 0x12000, v15\nM 88,224,136\nY 107,88,105\nL 80,52\n"
"M 92,248,136\nD 136,2,17664\nL 88,56\nM 92,252,140\nX 108,96,106\nX 109,107,106\nM 92,240,144\nG 36,11,0\nM 92,244,148\nY 110,104,80\nY 96,105,88\nM 96,232,148\nY 111,110,107\n"
"Y 112,110,96\nM 96,228,144\ns_waitcnt lgkmcnt(6)\nA 114,44\nA 115,48\nM 96,236,152\nG 40,11,1024\nM 96,224,140\nY 116,114,81\nY 117,89,115\nM 100,248,140\nD 140,2,17920\nA 81,52\n"
"M 100,252,144\nA 89,56\nX 118,97,116\nM 100,240,148\nX 119,117,116\nY 120,114,81\nM 100,244,152\nY 97,115,89\nY 121,120,117\nM 104,232,152\nY 122,120,97\nC 64,108,118\n"
"M 104,228,148\ns_waitcnt lgkmcnt(4)\nC 68,109,119\nC 72,111,121\nM 104,236,156\nC 76,112,122\nL 104,45\nM 104,224,144\nL 105,49\nY 106,104,82\nM 108,248,144\ns_waitcnt lgkmcnt(0)\n"
"s_barrier\nY 107,90,105\nM 108,252,148\nD 144,2,18176\nL 82,53\nM 108,240,152\nbuffer_store_dword v17, v23, s[40:43], 0 offen offset:8 sc1\nM 108,244,156\nL 90,57\nX 108,98,106\n"
"M 112,232,156\nX 109,107,106\nY 110,104,82\nM 112,228,152\nY 98,105,90\nY 111,110,107\nM 112,236,128\nY 112,110,98\nA 114,45\nM 112,224,148\nA 115,49\nY 116,114,83\nM 116,248,148\n"
"D 148,2,18432\nY 117,91,115\nM 116,252,152\nA 83,53\nA 91,57\nM 116,240,156\nX 118,99,116\nX 119,117,116\nM 116,244,128\nY 120,114,83\nY 99,115,91\nM 120,232,128\nK 232,-4096\n"
"Y 121,120,117\nM 120,228,156\nY 122,120,99\nC 65,108,118\nM 120,236,132\nC 69,109,119\nC 73,111,121\nM 120,224,152\nC 77,112,122\nL 104,46\nM 124,248,152\nD 152,2,18688\nL 105,50\n"
"M 124,252,156\nD 156,2,18944\nY 106,104,84\nM 124,240,128\nD 128,2,19200\nY 107,92,105\nM 124,244,132\ns_waitcnt vmcnt(22)\nD 132,2,19456\nL 84,54\nM 0,160,136\ns_waitcnt vmcnt(21)\n"
"K 228,-3072\nL 92,58\nM 0,164,140\ns_waitcnt vmcnt(21) lgkmcnt(5)\nX 108,100,106\nX 109,107,106\nM 0,168,144\ns_waitcnt vmcnt(20) lgkmcnt(4)\nY 110,104,84\nY 100,105,92\n"
"M 0,172,148\ns_waitcnt vmcnt(18)\nY 111,110,107\nY 112,110,100\nM 4,180,148\ns_waitcnt vmcnt(17)\nK 236,-2048\nA 114,46\nM 4,184,136\ns_waitcnt vmcnt(17)\nD 136,2,19744\nA 115,50\n"
"M 4,188,140\ns_waitcnt vmcnt(16)\nY 116,114,85\nY 117,93,115\nM 4,176,144\nA 85,54\nA 93,58\nM 8,164,144\nK 224,-1024\nX 118,101,116\nM 8,168,148\ns_waitcnt lgkmcnt(4)\n"
"X 119,117,116\nY 120,114,85\nM 8,172,152\nY 101,115,93\nY 121,120,117\nM 8,160,140\nY 122,120,101\nC 66,108,118\nM 12,184,140\nD 140,2,20000\nC 70,109,119\nM 12,188,144\nK 248,3072\n"
"C 74,111,121\nM 12,176,148\nC 78,112,122\nL 104,47\nM 12,180,152\nL 105,51\nY 106,104,86\nM 16,168,152\nY 107,94,105\nL 86,55\nM 16,164,148\ns_waitcnt lgkmcnt(4)\nK 252,0\nL 94,59\n"
"M 16,172,156\nX 108,102,106\nX 109,107,106\nM 16,160,144\nY 110,104,86\nY 102,105,94\nM 20,184,144\nD 144,2,20256\nY 111,110,107\nM 20,188,148\nK 240,1024\nY 112,110,102\n"
"M 20,176,152\nA 114,47\nA 115,51\nM 20,180,156\nY 116,114,87\nY 117,95,115\nM 24,168,156\nA 87,55\nA 95,59\nM 24,164,152\ns_waitcnt lgkmcnt(4)\nG 44,11,2048\nM 24,172,128\n"
"X 118,103,116\nX 119,117,116\nM 24,160,148\nY 120,114,87\nY 103,115,95\nM 28,184,148\nD 148,2,17728\nY 121,120,117\nM 28,188,152\nK 244,2048\nv_add_u32 v15, 0x2000, v15\n"
"M 28,176,156\nY 122,120,103\nC 67,108,118\nM 28,180,128\nC 71,109,119\nC 75,111,121\nM 32,168,128\nC 79,112,122\nL 104,60\nM 32,164,156\ns_waitcnt lgkmcnt(4)\nG 48,11,3072\n"
"M 32,172,132\nS 4,64,17664\nM 32,160,152\nL 105,24\nY 106,104,80\nM 36,184,152\ns_waitcnt vmcnt(22)\nY 107,88,105\nL 80,28\nM 36,188,156\ns_waitcnt vmcnt(14)\nD 152,2,17984\n"
"L 88,32\nM 36,176,128\nG 52,12,0\nM 36,180,132\nS 4,68,35328\nM 40,168,132\nX 108,96,106\nX 109,107,106\nM 40,164,128\ns_waitcnt lgkmcnt(6)\nY 110,104,80\nY 96,105,88\nM 40,172,136\n"
"G 56,12,1024\nM 40,160,156\nY 111,110,107\nY 112,110,96\nM 44,184,156\nD 156,2,18240\nA 114,60\nM 44,188,128\nS 5,72,35328\nM 44,176,132\nA 115,24\nY 116,114,81\nM 44,180,136\n"
"Y 117,89,115\nA 81,28\nM 48,168,136\nA 89,32\nX 118,97,116\nM 48,164,132\ns_waitcnt lgkmcnt(7)\nS 5,76,0\nM 48,172,140\nX 119,117,116\nY 120,114,81\nM 48,160,128\nY 97,115,89\n"
"Y 121,120,117\nM 52,184,128\nD 128,2,18496\nY 122,120,97\nM 52,188,132\nC 64,108,118\nC 68,109,119\nM 52,176,136\nC 72,111,121\nC 76,112,122\nM 52,180,140\nL 104,61\nL 105,25\n"
"M 56,168,140\nK 168,-4096\nY 106,104,82\nM 56,164,136\ns_waitcnt lgkmcnt(8)\nY 107,90,105\nL 82,29\nM 56,172,144\nL 90,33\nX 108,98,106\nM 56,160,132\n.Lpad8:\n"
".if (.Lpad8 - .Lbase) != 32780\n.error \"pad\"\n.endif\ns_cbranch_execz .Lpad12\nX 109,107,106\nY 110,104,82\nM 60,184,132\nD 132,2,18752\nY 98,105,90\nM 60,188,136\nD 136,2,19008\n"
"Y 111,110,107\nM 60,176,140\nD 140,2,19264\nY 112,110,98\nM 60,180,144\ns_waitcnt lgkmcnt(10)\nD 144,2,19520\nA 114,61\nM 0,200,148\ns_waitcnt lgkmcnt(9)\nK 164,-3072\nA 115,25\n"
"M 0,196,152\ns_waitcnt lgkmcnt(7)\nY 116,114,83\nY 117,91,115\nM 0,204,156\ns_waitcnt lgkmcnt(4)\nA 83,29\nA 91,33\nM 0,192,128\nX 118,99,116\nX 119,117,116\nM 4,216,128\n"
"K 172,-2048\nY 120,114,83\nM 4,220,148\nD 148,2,19808\nY 99,115,91\nM 4,208,152\ns_waitcnt vmcnt(18)\nY 121,120,117\nY 122,120,99\nM 4,212,156\nC 65,108,118\nC 69,109,119\n"
"M 8,196,156\nK 160,-1024\nC 73,111,121\nM 8,204,128\ns_waitcnt lgkmcnt(4)\nC 77,112,122\nL 104,62\nM 8,192,132\nL 105,26\nY 106,104,84\nM 8,200,152\nY 107,92,105\nL 84,30\n"
"M 12,220,152\nD 152,2,20064\nL 92,34\nM 12,208,156\nK 184,3072\nX 108,100,106\nM 12,212,128\nX 109,107,106\nY 110,104,84\nM 12,216,132\nY 100,105,92\nY 111,110,107\nM 16,204,132\n"
"Y 112,110,100\nA 114,62\nM 16,196,128\ns_waitcnt lgkmcnt(4)\nK 188,0\nA 115,26\nM 16,192,136\nY 116,114,85\nY 117,93,115\nM 16,200,156\nA 85,30\nA 93,34\nM 20,220,156\n"
"D 156,2,20320\nX 118,101,116\nM 20,208,128\nK 176,1024\nX 119,117,116\nM 20,212,132\nY 120,114,85\nY 101,115,93\nM 20,216,136\nY 121,120,117\nY 122,120,101\nM 24,204,136\n"
"C 66,108,118\nC 70,109,119\nM 24,196,132\ns_waitcnt lgkmcnt(4)\nK 180,2048\nv_add_u32 v15, 0xfffee000, v15\nM 24,192,140\nC 74,111,121\nC 78,112,122\nM 24,200,128\nL 104,63\n"
"L 105,27\nM 28,220,128\nD 128,0,0\nY 106,104,86\nM 28,208,132\nY 107,94,105\nL 86,31\nM 28,212,136\nL 94,35\nX 108,102,106\nM 28,216,140\nX 109,107,106\nY 110,104,86\nM 32,204,140\n"
"Y 102,105,94\nY 111,110,107\nM 32,196,136\ns_waitcnt lgkmcnt(4)\nY 112,110,102\nA 114,63\nM 32,192,144\nG 60,12,2048\nM 32,200,132\nA 115,27\nY 116,114,87\nM 36,220,132\n"
"D 132,0,256\nY 117,95,115\nM 36,208,136\nA 87,31\nA 95,35\nM 36,212,140\nG 24,12,3072\nM 36,216,144\nX 118,103,116\nX 119,117,116\nM 40,204,144\nY 120,114,87\nY 103,115,95\n"
"M 40,196,140\ns_waitcnt lgkmcnt(4)\nY 121,120,117\nY 122,120,103\nM 40,192,148\nH 17,13,0\nM 40,200,136\nC 67,108,118\nC 71,109,119\nM 44,220,136\nD 136,0,512\nC 75,111,121\n"
"M 44,208,140\nH 18,13,1024\nM 44,212,144\ns_waitcnt vmcnt(26)\nC 79,112,122\nL 104,36\nM 44,216,148\nH 19,13,2048\nM 48,204,148\nS 4,64,17920\nM 48,196,144\ns_waitcnt lgkmcnt(5)\n"
"H 20,13,3072\nM 48,192,152\ns_waitcnt vmcnt(27)\nL 105,40\nY 106,104,80\nM 48,200,140\nH 21,14,0\nM 52,220,140\nD 140,0,768\nY 107,88,105\nM 52,208,144\nH 22,14,1024\nM 52,212,148\n"
"s_waitcnt vmcnt(18)\nL 80,44\nL 88,48\nM 52,216,152\nH 23,14,2048\nM 56,204,152\nS 4,68,35584\nM 56,196,148\ns_waitcnt lgkmcnt(6)\nG 28,8,1152\nM 56,192,156\nX 108,96,106\n"
"X 109,107,106\nM 56,200,144\nY 110,104,80\nY 96,105,88\nM 60,220,144\nD 144,0,1024\nY 111,110,107\nM 60,208,148\nD 148,0,1280\nY 112,110,96\nM 60,212,152\nD 152,0,1536\nA 114,36\n"
"M 60,216,156\ns_waitcnt lgkmcnt(8)\nD 156,0,1792\nA 115,40\nM 64,232,128\ns_waitcnt lgkmcnt(8)\nG 32,8,2176\nM 64,228,132\ns_waitcnt lgkmcnt(7)\nS 5,72,35584\nM 64,236,136\n"
"s_waitcnt lgkmcnt(6)\nY 116,114,81\nY 117,89,115\nM 64,224,140\nA 81,44\nA 89,48\nM 68,248,140\nK 204,-4096\nX 118,97,116\nM 68,252,128\nD 128,0,2080\nX 119,117,116\nM 68,240,132\n"
"S 5,76,256\nM 68,244,136\nY 120,114,81\nY 97,115,89\nM 72,228,136\nK 196,-3072\nY 121,120,117\nM 72,236,140\ns_waitcnt lgkmcnt(6)\nY 122,120,97\nC 64,108,118\nM 72,224,144\n"
"C 68,109,119\nC 72,111,121\nM 72,232,132\nC 76,112,122\nL 104,37\nM 76,252,132\nD 132,0,2336\nL 105,41\nM 76,240,136\nK 192,-2048\nY 106,104,82\nM 76,244,140\nY 107,90,105\nL 82,45\n"
"M 76,248,144\nL 90,49\nX 108,98,106\nM 80,236,144\nX 109,107,106\nY 110,104,82\nM 80,228,140\ns_waitcnt lgkmcnt(6)\nK 200,-1024\nY 98,105,90\nM 80,224,148\nY 111,110,107\n"
"Y 112,110,98\nM 80,232,136\nA 114,37\nA 115,41\nM 84,252,136\nD 136,0,2592\nY 116,114,83\nM 84,240,140\nK 220,3072\nY 117,91,115\nM 84,244,144\nA 83,45\nA 91,49\nM 84,248,148\n"
"X 118,99,116\nX 119,117,116\nM 88,236,148\nY 120,114,83\nY 99,115,91\nM 88,228,144\ns_waitcnt lgkmcnt(6)\nK 208,0\nY 121,120,117\nM 88,224,152\nY 122,120,99\nC 65,108,118\n"
"M 88,232,140\nC 69,109,119\nC 73,111,121\nM 92,252,140\nD 140,0,64\nC 77,112,122\nM 92,240,144\nK 212,1024\nL 104,38\nM 92,244,148\nL 105,42\nY 106,104,84\nM 92,248,152\n"
"Y 107,92,105\nL 84,46\nM 96,236,152\nL 92,50\nX 108,100,106\nM 96,228,148\ns_waitcnt lgkmcnt(6)\nK 216,2048\nv_add_u32 v15, 0x2000, v15\nM 96,224,156\nX 109,107,106\nY 110,104,84\n"
"M 96,232,144\nY 100,105,92\nY 111,110,107\nM 100,252,144\nD 144,0,320\nY 112,110,100\nM 100,240,148\nA 114,38\nA 115,42\nM 100,244,152\nY 116,114,85\nY 117,93,115\nM 100,248,156\n"
"A 85,46\nA 93,50\nM 104,236,156\nX 118,101,116\nX 119,117,116\nM 104,228,152\ns_waitcnt lgkmcnt(5)\nY 120,114,85\nY 101,115,93\nM 104,224,128\n"
"buffer_load_dword v124, v127, s[40:43], 0 offen offset:8 sc1\nM 104,232,148\nY 121,120,117\nY 122,120,101\nM 108,252,148\nD 148,0,576\nC 66,108,118\nM 108,240,152\nC 70,109,119\n"
"C 74,111,121\nM 108,244,156\nC 78,112,122\nL 104,39\nM 108,248,128\nL 105,43\nY 106,104,86\nM 112,236,128\nY 107,94,105\nL 86,47\nM 112,228,156\ns_waitcnt lgkmcnt(4)\nL 94,51\n"
"X 108,102,106\nM 112,224,132\nX 109,107,106\nY 110,104,86\nM 112,232,152\nY 102,105,94\nY 111,110,107\nM 116,252,152\nD 152,0,832\nY 112,110,102\nM 116,240,156\nA 114,39\nA 115,43\n"
"M 116,244,128\nG 36,8,3200\nM 116,248,132\nY 116,114,87\nY 117,95,115\nM 120,236,132\nA 87,47\nA 95,51\nM 120,228,128\ns_waitcnt lgkmcnt(4)\nX 118,103,116\nX 119,117,116\n"
"M 120,224,136\nG 40,9,128\nM 120,232,156\nY 120,114,87\nY 103,115,95\nM 124,252,156\nD 156,0,1088\nY 121,120,117\nM 124,240,128\nD 128,0,1344\nY 122,120,103\nM 124,244,132\n"
"D 132,0,1600\nC 67,108,118\nM 124,248,136\ns_waitcnt vmcnt(29) lgkmcnt(6)\nD 136,0,1856\nC 71,109,119\nM 64,168,140\ns_waitcnt vmcnt(28) lgkmcnt(6)\nG 44,9,1152\nM 64,164,144\n"
"s_waitcnt vmcnt(28) lgkmcnt(5)\nC 75,111,121\nC 79,112,122\nM 64,172,148\ns_waitcnt vmcnt(27) lgkmcnt(4)\nS 4,64,18176\nM 64,160,152\ns_waitcnt vmcnt(26)\nL 104,52\nL 105,56\n"
"M 68,184,152\ns_waitcnt vmcnt(25)\nG 48,9,2176\nM 68,188,140\ns_waitcnt vmcnt(25)\nD 140,0,2144\nY 106,104,80\nM 68,176,144\ns_waitcnt vmcnt(23)\nY 107,88,105\nL 80,60\n"
"M 68,180,148\ns_waitcnt vmcnt(22)\nL 88,24\nX 108,96,106\nM 72,164,148\nK 236,-4096\nX 109,107,106\nM 72,172,152\ns_waitcnt lgkmcnt(5)\nS 4,68,35840\nM 72,160,156\nY 110,104,80\n"
"Y 96,105,88\nM 72,168,144\nY 111,110,107\nY 112,110,96\nM 76,188,144\nD 144,0,2400\nA 114,52\nM 76,176,148\nK 228,-3072\nA 115,56\nM 76,180,152\nS 5,72,35840\nM 76,184,156\n"
"Y 116,114,81\nY 117,89,115\nM 80,172,156\nA 81,60\nA 89,24\nM 80,164,152\ns_waitcnt lgkmcnt(7)\nK 224,-2048\nX 118,97,116\nM 80,160,128\nS 5,76,512\nM 80,168,148\nX 119,117,116\n"
"Y 120,114,81\nM 84,188,148\nY 97,115,89\nY 121,120,117\nM 84,176,152\nD 148,0,2656\nY 122,120,97\nM 84,180,156\nK 232,-1024\nC 64,108,118\nM 84,184,128\nC 68,109,119\nC 72,111,121\n"
"M 88,172,128\nC 76,112,122\nL 104,53\nM 88,164,156\ns_waitcnt lgkmcnt(8)\nL 105,57\nY 106,104,82\nM 88,160,132\nK 252,3072\nY 107,90,105\nM 88,168,152\nL 82,61\nL 90,25\n"
"M 92,188,152\nD 152,0,17664\nX 108,98,106\nM 92,176,156\nX 109,107,106\nY 110,104,82\nM 92,180,128\nK 240,0\nY 98,105,90\nM 92,184,132\nY 111,110,107\nY 112,110,98\nM 96,172,132\n"
"A 114,53\nA 115,57\nM 96,164,128\ns_waitcnt lgkmcnt(8)\nY 116,114,83\nY 117,91,115\nM 96,160,136\nK 244,1024\nA 83,61\nM 96,168,156\nA 91,25\nX 118,99,116\nM 100,188,156\n"
"D 156,0,17920\nX 119,117,116\nM 100,176,128\nY 120,114,83\nY 99,115,91\nM 100,180,132\nK 248,2048\nv_add_u32 v15, 0x2000, v15\nM 100,184,136\ns_waitcnt vmcnt(12)\n"
"v_cmp_eq_u32 vcc, v124, v126\ns_cbranch_vccz 2f\ns_mov_b32 s47, 0x400000\n1:\nbuffer_load_dword v124, v127, s[40:43], 0 offen offset:8 sc1\ns_waitcnt vmcnt(0)\n"
"v_cmp_eq_u32 vcc, v124, v126\ns_cbranch_vccz 2f\ns_sub_u32 s47, s47, 1\ns_cbranch_scc0 1b\n2:\nM 104,172,136\nY 121,120,117\nY 122,120,99\nM 104,164,132\ns_waitcnt lgkmcnt(7)\n"
"C 65,108,118\nC 69,109,119\nM 104,160,140\nC 73,111,121\nC 77,112,122\nM 104,168,128\nL 104,54\nL 105,58\nM 108,188,128\nD 128,0,18176\nY 106,104,84\nM 108,176,132\nY 107,92,105\n"
"L 84,62\nM 108,180,136\nL 92,26\nX 108,100,106\nM 108,184,140\nX 109,107,106\nY 110,104,84\nM 112,172,140\nY 100,105,92\nY 111,110,107\nM 112,164,136\ns_waitcnt lgkmcnt(6)\n"
"Y 112,110,100\nA 114,54\nM 112,160,144\nA 115,58\nY 116,114,85\nM 112,168,132\nY 117,93,115\nA 85,62\nM 116,188,132\nD 132,0,18432\nA 93,26\nM 116,176,136\nX 118,101,116\n"
"X 119,117,116\nM 116,180,140\nY 120,114,85\nY 101,115,93\nM 116,184,144\nY 121,120,117\nY 122,120,101\nM 120,172,144\nK 172,-4096\nC 66,108,118\nM 120,164,140\ns_waitcnt lgkmcnt(4)\n"
"C 70,109,119\nC 74,111,121\nM 120,160,148\nC 78,112,122\nL 104,55\nM 120,168,136\nL 105,59\nY 106,104,86\nM 124,188,136\nD 136,0,18688\nY 107,94,105\nM 124,176,140\nD 140,0,18944\n"
"L 86,63\nM 124,180,144\nD 144,0,19200\nL 94,27\nM 124,184,148\ns_waitcnt lgkmcnt(6)\nD 148,0,19456\nX 108,102,106\nM 0,204,152\ns_waitcnt lgkmcnt(6)\nK 164,-3072\nX 109,107,106\n"
"M 0,196,156\ns_waitcnt lgkmcnt(5)\nY 110,104,86\nY 102,105,94\nM 0,192,128\ns_waitcnt lgkmcnt(4)\nY 111,110,107\nY 112,110,102\nM 0,200,132\nA 114,55\nA 115,59\nM 4,220,132\n"
"G 52,9,3200\nM 4,208,152\nD 152,0,19744\nY 116,114,87\nM 4,212,156\nY 117,95,115\nA 87,63\nM 4,216,128\nA 95,27\nX 118,103,116\nM 8,196,128\nG 56,10,128\nM 8,192,132\n"
"s_waitcnt lgkmcnt(4)\nX 119,117,116\nY 120,114,87\nM 8,200,136\nY 103,115,95\nY 121,120,117\nM 8,204,156\nY 122,120,103\nC 67,108,118\nM 12,208,156\nD 156,0,20000\nC 71,109,119\n"
"M 12,212,128\nG 60,10,1152\nM 12,216,132\nC 75,111,121\nC 79,112,122\nM 12,220,136\nS 4,64,18432\nM 16,192,136\n.Lpad9:\n.if (.Lpad9 - .Lbase) != 36864\n.error \"pad\"\n.endif\n"
"s_cbranch_execz .Lpad13\nL 80,17\nL 81,18\nM 16,196,132\ns_waitcnt lgkmcnt(5)\nG 24,10,2176\nM 16,200,140\nL 82,19\nY 80,80,82\nM 16,204,128\nL 104,20\nL 105,21\nM 20,208,128\n"
"D 128,0,20256\nY 106,104,81\nM 20,212,132\nK 160,-2048\nY 107,82,105\nM 20,216,136\nS 4,68,36096\nM 20,220,140\nL 81,22\nL 82,23\nM 24,192,140\nX 108,80,106\nX 109,107,106\n"
"M 24,196,136\ns_waitcnt lgkmcnt(6)\nK 168,-1024\nY 110,104,81\nM 24,200,144\nY 80,105,82\nY 111,110,107\nM 24,204,132\nY 112,110,80\nA 84,17\nM 28,208,132\nD 132,0,17728\nA 85,18\n"
"M 28,212,136\nK 188,3072\nA 86,19\nM 28,216,140\nS 5,72,36096\nM 28,220,144\nY 84,84,86\nA 114,20\nM 32,192,144\nA 115,21\nY 116,114,85\nM 32,196,140\ns_waitcnt lgkmcnt(7)\nK 176,0\n"
"Y 117,86,115\nM 32,200,148\nS 5,76,768\nM 32,204,136\nA 85,22\nA 86,23\nM 36,208,136\nX 118,84,116\nX 119,117,116\nM 36,212,140\nD 136,0,17984\nY 120,114,85\nM 36,216,144\n"
"K 180,1024\nY 84,115,86\nM 36,220,148\nY 121,120,117\nY 122,120,84\nM 40,192,148\nC 64,108,118\nC 65,109,119\nM 40,196,144\ns_waitcnt lgkmcnt(8)\nC 66,111,121\nC 67,112,122\n"
"M 40,200,152\nK 184,2048\nv_add_u32 v15, 0x2000, v15\nM 40,204,140\nU 7,64,0\nM 44,208,140\nD 140,0,18240\nL 104,28\nM 44,212,144\nU 7,65,17664\nM 44,216,148\nU 123,66,0\n"
"M 44,220,152\nU 7,67,35328\nM 48,192,152\ns_waitcnt vmcnt(23)\nL 80,32\nL 88,36\nM 48,196,148\ns_waitcnt lgkmcnt(12)\nY 96,104,88\nA 105,28\nM 48,200,156\nA 81,32\nA 89,36\n"
"M 48,204,144\nY 97,105,89\nL 104,29\nM 52,208,144\nD 144,0,18496\nL 82,33\nM 52,212,148\nL 90,37\nY 98,104,90\nM 52,216,152\nA 105,29\nA 83,33\nM 52,220,156\nA 91,37\nY 99,105,91\n"
"M 56,192,156\nK 192,-4096\nL 104,30\nM 56,196,152\ns_waitcnt lgkmcnt(11)\nL 84,34\nL 92,38\nM 56,200,128\nY 100,104,92\nA 105,30\nM 56,204,148\nA 85,34\nA 93,38\nM 60,208,148\n"
"D 148,0,18752\nY 101,105,93\nM 60,212,152\nD 152,0,19008\nL 104,31\nM 60,216,156\nD 156,0,19264\nL 86,35\nM 60,220,128\ns_waitcnt vmcnt(20) lgkmcnt(12)\nD 128,0,19520\nL 94,39\n"
"M 0,236,132\ns_waitcnt vmcnt(19) lgkmcnt(10)\nK 196,-3072\nY 102,104,94\nM 0,228,136\ns_waitcnt vmcnt(19) lgkmcnt(8)\nA 105,31\nA 87,35\nM 0,224,140\ns_waitcnt vmcnt(18) lgkmcnt(4)\n"
"A 95,39\nY 103,105,95\nM 0,232,144\ns_waitcnt vmcnt(17)\nL 104,40\nL 105,44\nM 4,252,144\ns_waitcnt vmcnt(16)\nG 28,10,3200\nM 4,240,132\ns_waitcnt vmcnt(16)\nD 132,0,19808\n"
"Y 106,104,80\nM 4,244,136\ns_waitcnt vmcnt(15)\nY 107,88,105\nL 80,48\nM 4,248,140\ns_waitcnt vmcnt(12)\nL 88,52\nX 108,96,106\nM 8,228,140\nG 32,11,128\nM 8,224,144\n"
"s_waitcnt lgkmcnt(4)\nX 109,107,106\nY 110,104,80\nM 8,232,148\nY 96,105,88\nY 111,110,107\nM 8,236,136\nY 112,110,96\nA 114,40\nM 12,240,136\nD 136,0,20064\nA 115,44\nM 12,244,140\n"
"G 36,11,1152\nM 12,248,144\nY 116,114,81\nY 117,89,115\nM 12,252,148\nA 81,48\nA 89,52\nM 16,224,148\nX 118,97,116\nX 119,117,116\nM 16,228,144\ns_waitcnt lgkmcnt(4)\nK 200,-2048\n"
"Y 120,114,81\nM 16,232,152\nY 97,115,89\nY 121,120,117\nM 16,236,140\nY 122,120,97\nC 64,108,118\nM 20,240,140\nD 140,0,20320\nC 124,109,119\nM 20,244,144\nK 204,-1024\n"
"C 72,111,121\nM 20,248,148\nC 76,112,122\nL 104,41\nM 20,252,152\nL 105,45\nY 106,104,82\nM 24,224,152\nY 107,90,105\nL 82,49\nM 24,228,148\ns_waitcnt lgkmcnt(0)\ns_barrier\n"
"L 90,53\nM 24,232,156\nK 208,3072\nX 108,98,106\nM 24,236,144\nX 109,107,106\nY 110,104,82\nM 28,240,144\nD 144,0,35328\nY 98,105,90\nM 28,244,148\nY 111,110,107\nY 112,110,98\n"
"M 28,248,152\nK 212,0\nA 114,41\nM 28,252,156\nA 115,45\nY 116,114,83\nM 32,224,156\nY 117,91,115\nA 83,49\nM 32,228,152\nA 91,53\nX 118,99,116\nM 32,232,128\nK 216,1024\n"
"X 119,117,116\nM 32,236,148\nY 120,114,83\nY 99,115,91\nM 36,240,148\nD 148,0,35584\nY 121,120,117\nM 36,244,152\nY 122,120,99\nC 65,108,118\nM 36,248,156\nK 220,2048\n"
"v_add_u32 v15, 0xe000, v15\nM 36,252,128\nC 125,109,119\nC 73,111,121\nM 40,224,128\nC 77,112,122\nL 104,42\nM 40,228,156\nL 105,46\nY 106,104,84\nM 40,232,132\nY 107,92,105\n"
"L 84,50\nM 40,236,152\nL 92,54\nX 108,100,106\nM 44,240,152\nD 152,0,35840\nX 109,107,106\nM 44,244,156\nY 110,104,84\nY 100,105,92\nM 44,248,128\nY 111,110,107\nY 112,110,100\n"
"M 44,252,132\nA 114,42\nA 115,46\nM 48,224,132\nY 116,114,85\nY 117,93,115\nM 48,228,128\nA 85,50\nA 93,54\nM 48,232,136\nX 118,101,116\nX 119,117,116\nM 48,236,156\nY 120,114,85\n"
"Y 101,115,93\nM 52,240,156\nD 156,0,36096\nY 121,120,117\nM 52,244,128\nY 122,120,101\nC 66,108,118\nM 52,248,132\nC 126,109,119\nC 74,111,121\nM 52,252,136\nC 78,112,122\nL 104,43\n"
"M 56,224,136\nK 224,-4096\nL 105,47\nM 56,228,132\nY 106,104,86\nY 107,94,105\nM 56,232,140\nL 86,51\nL 94,55\nM 56,236,128\nX 108,102,106\nX 109,107,106\nM 60,240,128\n"
"D 128,0,36352\nY 110,104,86\nM 60,244,132\nD 132,0,36608\nY 102,105,94\nM 60,248,136\nD 136,0,36864\nY 111,110,107\nM 60,252,140\ns_waitcnt lgkmcnt(6)\nD 140,0,37120\nY 112,110,102\n"
"M 0,172,144\ns_waitcnt lgkmcnt(6)\nK 228,-3072\nA 114,43\nM 0,164,148\ns_waitcnt vmcnt(18) lgkmcnt(5)\nA 115,47\nY 116,114,87\nM 0,160,152\ns_waitcnt vmcnt(17) lgkmcnt(4)\n"
"Y 117,95,115\nA 87,51\nM 0,168,156\ns_waitcnt vmcnt(16)\nA 95,55\nX 118,103,116\nM 4,188,156\ns_waitcnt vmcnt(15)\nG 40,11,2176\nM 4,176,144\ns_waitcnt vmcnt(15)\nD 144,0,37408\n"
"X 119,117,116\nM 4,180,148\ns_waitcnt vmcnt(14)\nY 120,114,87\nY 103,115,95\nM 4,184,152\nY 121,120,117\nY 122,120,103\nM 8,164,152\nG 44,11,3200\nM 8,160,156\ns_waitcnt lgkmcnt(4)\n"
"C 67,108,118\nC 127,109,119\nM 8,168,128\nC 75,111,121\nC 79,112,122\nM 8,172,148\nL 104,56\nL 105,60\nM 12,176,148\nD 148,0,37664\nY 106,104,80\nM 12,180,152\nG 48,12,128\n"
"M 12,184,156\nS 3,64,0\nM 12,188,128\nY 107,88,105\nL 80,24\nM 16,160,128\ns_waitcnt vmcnt(13)\nL 88,28\nX 108,96,106\nM 16,164,156\ns_waitcnt lgkmcnt(5)\nG 52,12,1152\n"
"M 16,168,132\nX 109,107,106\nY 110,104,80\nM 16,172,152\nY 96,105,88\nY 111,110,107\nM 20,176,152\nD 152,0,37920\nY 112,110,96\nM 20,180,156\nK 232,-2048\nA 114,56\nM 20,184,128\n"
"S 5,72,17664\nM 20,188,132\nA 115,60\nY 116,114,81\nM 24,160,132\nY 117,89,115\nA 81,24\nM 24,164,128\ns_waitcnt lgkmcnt(6)\nK 236,-1024\nA 89,28\nM 24,168,136\nS 3,76,17664\n"
"M 24,172,156\nX 118,97,116\nX 119,117,116\nM 28,176,156\nY 120,114,81\nY 97,115,89\nM 28,180,128\nD 156,0,35392\nY 121,120,117\nM 28,184,132\nK 240,3072\nY 122,120,97\nM 28,188,136\n"
"C 64,108,118\nC 68,109,119\nM 32,160,136\nC 72,111,121\nC 76,112,122\nM 32,164,132\ns_waitcnt lgkmcnt(7)\nL 104,57\nL 105,61\nM 32,168,140\nK 244,0\nY 106,104,82\nM 32,172,128\n"
"Y 107,90,105\nL 82,25\nM 36,176,128\nD 128,0,35648\nL 90,29\nM 36,180,132\nX 108,98,106\nX 109,107,106\nM 36,184,136\nK 248,1024\nY 110,104,82\nM 36,188,140\nY 98,105,90\n"
"Y 111,110,107\nM 40,160,140\nY 112,110,98\nA 114,57\nM 40,164,136\ns_waitcnt lgkmcnt(7)\nA 115,61\nY 116,114,83\nM 40,168,144\nK 252,2048\nv_add_u32 v15, 0x2000, v15\nM 40,172,132\n"
"Y 117,91,115\nA 83,25\nM 44,176,132\nD 132,0,35904\nA 91,29\nM 44,180,136\nX 118,99,116\nX 119,117,116\nM 44,184,140\nY 120,114,83\nY 99,115,91\nM 44,188,144\nY 121,120,117\n"
"Y 122,120,99\nM 48,160,144\nC 65,108,118\nC 69,109,119\nM 48,164,140\ns_waitcnt lgkmcnt(7)\nC 73,111,121\nC 77,112,122\nM 48,168,148\nL 104,58\nL 105,62\nM 48,172,136\nY 106,104,84\n"
"Y 107,92,105\nM 52,176,136\nD 136,0,36160\nL 84,26\nM 52,180,140\nL 92,30\nX 108,100,106\nM 52,184,144\nX 109,107,106\nY 110,104,84\nM 52,188,148\nY 100,105,92\nY 111,110,107\n"
"M 56,160,148\nK 160,-4096\nY 112,110,100\nM 56,164,144\ns_waitcnt lgkmcnt(6)\nA 114,58\nA 115,62\nM 56,168,152\nY 116,114,85\nY 117,93,115\nM 56,172,140\nA 85,26\nA 93,30\n"
"M 60,176,140\nD 140,0,36416\nX 118,101,116\nM 60,180,144\nD 144,0,36672\nX 119,117,116\nM 60,184,148\nD 148,0,36928\nY 120,114,85\nM 60,188,152\ns_waitcnt lgkmcnt(6)\nD 152,0,37184\n"
"Y 101,115,93\nM 0,192,156\ns_waitcnt lgkmcnt(6)\nK 164,-3072\nY 121,120,117\nM 0,196,128\ns_waitcnt vmcnt(19) lgkmcnt(5)\nY 122,120,101\nC 66,108,118\nM 0,200,132\n"
"s_waitcnt vmcnt(18) lgkmcnt(4)\nC 70,109,119\nC 74,111,121\nM 0,204,136\ns_waitcnt vmcnt(17)\nC 78,112,122\nL 104,59\nM 4,208,136\ns_waitcnt vmcnt(16)\nK 168,-2048\nL 105,63\n"
"M 4,212,156\ns_waitcnt vmcnt(16)\nD 156,0,37472\nY 106,104,86\nM 4,216,128\ns_waitcnt vmcnt(15)\nY 107,94,105\nL 86,27\nM 4,220,132\nRA 128,0\nX 128,128,192\nM 8,196,132\n"
"K 172,-1024\nRA 129,1\nM 8,200,136\ns_waitcnt lgkmcnt(4)\nX 129,129,193\nRA 130,2\nM 8,204,140\nX 130,130,194\nRA 131,3\nM 8,192,128\nX 131,131,195\nRA 132,4\nM 12,212,128\n"
"D 128,0,37728\nX 132,132,196\nM 12,216,132\nK 176,3072\nRA 133,5\nM 12,220,136\nX 133,133,197\nRA 134,6\nM 12,208,140\nX 134,134,198\nRA 135,7\nM 16,200,140\nX 135,135,199\n"
"RA 136,8\nM 16,196,136\ns_waitcnt lgkmcnt(4)\nK 180,0\nX 136,136,200\nM 16,204,144\nRA 137,9\nX 137,137,201\nM 16,192,132\nRA 138,10\nX 138,138,202\nM 20,212,132\nD 132,0,37984\n"
"RA 139,11\nM 20,216,136\nK 184,1024\nX 139,139,203\nM 20,220,140\nRA 140,12\nX 140,140,204\nM 20,208,144\nRA 141,13\nX 141,141,205\nM 24,200,144\nRA 142,14\nX 142,142,206\n"
"M 24,196,140\ns_waitcnt lgkmcnt(0)\ns_barrier\nRA 143,15\nM 24,204,148\nK 188,2048\nv_add_u32 v15, 0x2000, v15\nM 24,192,136\nX 143,143,207\nRA 144,16\nM 28,212,136\nD 136,1,0\n"
"X 144,144,208\nM 28,216,140\nRA 145,17\nX 145,145,209\nM 28,220,144\nRA 146,18\nX 146,146,210\nM 28,208,148\nRA 147,19\nX 147,147,211\nM 32,200,148\nRA 148,20\nX 148,148,212\n"
"M 32,196,144\nRA 149,21\nX 149,149,213\nM 32,204,152\nRA 150,22\nX 150,150,214\nM 32,192,140\nRA 151,23\nX 151,151,215\nM 36,212,140\nD 140,1,256\nRA 152,24\nM 36,216,144\n.Lpad10:\n"
".if (.Lpad10 - .Lbase) != 40968\n.error \"pad\"\n.endif\ns_cbranch_execz .Lpad14\nX 152,152,216\nRA 153,25\nM 36,220,148\nX 153,153,217\nRA 154,26\nM 36,208,152\nX 154,154,218\n"
"RA 155,27\nM 40,200,152\nX 155,155,219\nRA 156,28\nM 40,196,148\nX 156,156,220\nRA 157,29\nM 40,204,156\nX 157,157,221\nRA 158,30\nM 40,192,144\nX 158,158,222\nRA 159,31\n"
"M 44,212,144\nD 144,1,512\nX 159,159,223\nM 44,216,148\nRA 160,32\nX 160,160,224\nM 44,220,152\nRA 161,33\nX 161,161,225\nM 44,208,156\nRA 162,34\nX 162,162,226\nM 48,200,156\n"
"RA 163,35\nX 163,163,227\nM 48,196,152\nRA 164,36\nX 164,164,228\nM 48,204,128\nRA 165,37\nX 165,165,229\nM 48,192,148\nRA 166,38\nX 166,166,230\nM 52,212,148\nD 148,1,768\n"
"RA 167,39\nM 52,216,152\nX 167,167,231\nRA 168,40\nM 52,220,156\nX 168,168,232\nRA 169,41\nM 52,208,128\nX 169,169,233\nRA 170,42\nM 56,200,128\nK 200,-3072\nX 170,170,234\n"
"M 56,196,156\nRA 171,43\nX 171,171,235\nM 56,204,132\nRA 172,44\nX 172,172,236\nM 56,192,152\nRA 173,45\nX 173,173,237\nM 60,212,152\nD 152,1,1024\nRA 174,46\nM 60,216,156\n"
"D 156,1,1280\nX 174,174,238\nM 60,220,128\nD 128,1,1536\nRA 175,47\nM 60,208,132\ns_waitcnt vmcnt(20) lgkmcnt(6)\nD 132,1,1792\nX 175,175,239\nM 64,224,136\n"
"s_waitcnt vmcnt(19) lgkmcnt(6)\nK 196,-2048\nRA 176,48\nM 64,228,140\ns_waitcnt vmcnt(15) lgkmcnt(5)\nX 176,176,240\nRA 177,49\nM 64,232,144\ns_waitcnt vmcnt(14) lgkmcnt(4)\n"
"X 177,177,241\nRA 178,50\nM 64,236,148\ns_waitcnt vmcnt(13)\nX 178,178,242\nRA 179,51\nM 68,240,148\ns_waitcnt vmcnt(12)\nK 204,-1024\nX 179,179,243\nM 68,244,136\n"
"s_waitcnt vmcnt(12)\nD 136,1,2080\nRA 180,52\nM 68,248,140\ns_waitcnt vmcnt(11)\nX 180,180,244\nRA 181,53\nM 68,252,144\nX 181,181,245\nRA 182,54\nM 72,228,144\nK 192,3072\n"
"X 182,182,246\nM 72,232,148\ns_waitcnt lgkmcnt(4)\nRA 183,55\nX 183,183,247\nM 72,236,152\nRA 184,56\nX 184,184,248\nM 72,224,140\nRA 185,57\nX 185,185,249\nM 76,244,140\n"
"D 140,1,2336\nRA 186,58\nM 76,248,144\nK 212,1024\nX 186,186,250\nM 76,252,148\nRA 187,59\nX 187,187,251\nM 76,240,152\nRA 188,60\nX 188,188,252\nM 80,232,152\nRA 189,61\n"
"X 189,189,253\nM 80,228,148\ns_waitcnt lgkmcnt(4)\nK 216,2048\nv_add_u32 v15, 0x2000, v15\nM 80,236,156\nRA 190,62\nX 190,190,254\nM 80,224,144\nRA 191,63\nX 191,191,255\n"
"M 84,244,144\nD 144,1,2592\nL 94,31\nM 84,248,148\nK 220,-3072\nX 108,102,106\nM 84,252,152\nX 109,107,106\nY 110,104,86\nM 84,240,156\nY 102,105,94\nY 111,110,107\nM 88,232,156\n"
"Y 112,110,102\nA 114,59\nM 88,228,152\ns_waitcnt lgkmcnt(4)\nG 56,12,2176\nM 88,236,128\nA 115,63\nY 116,114,87\nM 88,224,148\nY 117,95,115\nA 87,27\nM 92,244,148\nD 148,1,64\n"
"A 95,31\nM 92,248,152\nG 60,12,3200\nM 92,252,156\nX 118,103,116\nX 119,117,116\nM 92,240,128\nY 120,114,87\nY 103,115,95\nM 96,232,128\nY 121,120,117\nY 122,120,103\nM 96,228,156\n"
"s_waitcnt lgkmcnt(4)\nH 17,13,128\nM 96,236,132\nC 67,108,118\nC 71,109,119\nM 96,224,152\nH 18,13,1152\nM 100,244,152\nD 152,1,320\nC 75,111,121\nM 100,248,156\nH 19,13,2176\n"
"M 100,252,128\nC 79,112,122\nL 104,32\nM 100,240,132\nH 20,13,3200\nM 104,232,132\nS 3,64,256\nM 104,228,128\ns_waitcnt lgkmcnt(5)\nH 21,14,128\nM 104,236,136\nL 105,36\n"
"Y 106,104,80\nM 104,224,156\nH 22,14,1152\nM 108,244,156\nD 156,1,576\nY 107,88,105\nM 108,248,128\nH 23,14,2176\nM 108,252,132\nS 5,72,17920\nM 108,240,136\nG 24,8,1280\n"
"M 112,232,136\nL 80,40\nL 88,44\nM 112,228,132\ns_waitcnt lgkmcnt(6)\nX 108,96,106\nX 109,107,106\nM 112,236,140\nS 3,76,17920\nM 112,224,128\nG 28,8,2304\nM 116,244,128\n"
"Y 110,104,80\nY 96,105,88\nM 116,248,132\nD 128,1,832\nY 111,110,107\nM 116,252,136\nY 112,110,96\nA 114,32\nM 116,240,140\nK 208,-2048\nA 115,36\nM 120,232,140\nS 3,68,35584\n"
"M 120,228,136\ns_waitcnt lgkmcnt(8)\nY 116,114,81\nY 117,89,115\nM 120,236,144\nA 81,40\nA 89,44\nM 120,224,132\nK 232,-1024\nX 118,97,116\nM 124,244,132\nD 132,1,1088\n"
"X 119,117,116\nM 124,248,136\nD 136,1,1344\nY 120,114,81\nM 124,252,140\nD 140,1,1600\nY 97,115,89\nM 124,240,144\ns_waitcnt vmcnt(27) lgkmcnt(10)\nD 144,1,1856\nY 121,120,117\n"
"M 64,160,148\ns_waitcnt vmcnt(26) lgkmcnt(10)\nK 228,3072\nY 122,120,97\nM 64,164,152\ns_waitcnt vmcnt(26) lgkmcnt(8)\nS 3,124,35328\nM 64,168,156\ns_waitcnt vmcnt(25) lgkmcnt(6)\n"
"C 64,108,118\nC 68,109,119\nM 64,172,128\ns_waitcnt vmcnt(24)\nC 72,111,121\nC 76,112,122\nM 68,176,128\ns_waitcnt vmcnt(23)\nK 236,1024\nL 104,33\nM 68,180,148\n"
"s_waitcnt vmcnt(23)\nD 148,1,2144\nL 105,37\nM 68,184,152\ns_waitcnt vmcnt(22)\nY 106,104,82\nY 107,90,105\nM 68,188,156\nRA 113,64\nY 192,113,192\nM 72,164,156\nK 224,2048\n"
"v_add_u32 v15, 0xe000, v15\nM 72,168,128\ns_waitcnt lgkmcnt(5)\nRA 113,65\nY 193,113,193\nM 72,172,132\nRA 113,66\nY 194,113,194\nM 72,160,152\nRA 113,67\nY 195,113,195\n"
"M 76,180,152\nD 152,1,2400\nRA 113,68\nM 76,184,156\nK 244,-4096\nY 196,113,196\nM 76,188,128\nRA 113,69\nY 197,113,197\nM 76,176,132\nRA 113,70\nY 198,113,198\nM 80,168,132\n"
"RA 113,71\nY 199,113,199\nM 80,164,128\ns_waitcnt lgkmcnt(5)\nK 248,-3072\nRA 113,72\nM 80,172,136\nY 200,113,200\nRA 113,73\nM 80,160,156\nY 201,113,201\nRA 113,74\nM 84,180,156\n"
"D 156,1,2656\nY 202,113,202\nM 84,184,128\nK 252,-2048\nRA 113,75\nM 84,188,132\nY 203,113,203\nRA 113,76\nM 84,176,136\nY 204,113,204\nRA 113,77\nM 88,168,136\nY 205,113,205\n"
"RA 113,78\nM 88,164,132\ns_waitcnt lgkmcnt(5)\nK 240,-1024\nY 206,113,206\nM 88,172,140\nRA 113,79\nY 207,113,207\nM 88,160,128\nRA 113,80\nY 208,113,208\nM 92,180,128\n"
"D 128,1,17920\nRA 113,81\nM 92,184,132\nY 209,113,209\nRA 113,82\nM 92,188,136\nY 210,113,210\nRA 113,83\nM 92,176,140\nY 211,113,211\nRA 113,84\nM 96,168,140\nY 212,113,212\n"
"RA 113,85\nM 96,164,136\ns_waitcnt lgkmcnt(5)\nY 213,113,213\nRA 113,86\nM 96,172,144\nY 214,113,214\nRA 113,87\nM 96,160,132\nY 215,113,215\nRA 113,88\nM 100,180,132\n"
"D 132,1,18176\nY 216,113,216\nM 100,184,136\nRA 113,89\nY 217,113,217\nM 100,188,140\nRA 113,90\nY 218,113,218\nM 100,176,144\nRA 113,91\nY 219,113,219\nM 104,168,144\nRA 113,92\n"
"Y 220,113,220\nM 104,164,140\ns_waitcnt lgkmcnt(4)\nRA 113,93\nY 221,113,221\nM 104,172,148\nRA 113,94\nY 222,113,222\nM 104,160,136\nRA 113,95\nY 223,113,223\nM 108,180,136\n"
"D 136,1,18432\nRA 113,96\nM 108,184,140\nY 224,113,224\nRA 113,97\nM 108,188,144\nY 225,113,225\nRA 113,98\nM 108,176,148\nY 226,113,226\nRA 113,99\nM 112,168,148\nY 227,113,227\n"
"RA 113,100\nM 112,164,144\ns_waitcnt lgkmcnt(4)\nY 228,113,228\nRA 113,101\nM 112,172,152\nY 229,113,229\nRA 113,102\nM 112,160,140\nY 230,113,230\nRA 113,103\nM 116,180,140\n"
"D 140,1,18688\nY 231,113,231\nM 116,184,144\nRA 113,104\nY 232,113,232\nM 116,188,148\nRA 113,105\nY 233,113,233\nM 116,176,152\nRA 113,106\nY 234,113,234\nM 120,168,152\n"
"K 168,3072\nRA 113,107\nM 120,164,148\ns_waitcnt lgkmcnt(4)\nY 235,113,235\nRA 113,108\nM 120,172,156\nY 236,113,236\nRA 113,109\nM 120,160,144\nY 237,113,237\nRA 113,110\n"
"M 124,180,144\nD 144,1,18944\nY 238,113,238\nM 124,184,148\nD 148,1,19200\nRA 113,111\nM 124,188,152\nD 152,1,19456\nY 239,113,239\nM 124,176,156\ns_waitcnt vmcnt(27) lgkmcnt(6)\n"
"D 156,1,19744\nRA 113,112\nM 0,200,128\ns_waitcnt vmcnt(26) lgkmcnt(6)\nK 164,0\nY 240,113,240\nM 0,196,132\ns_waitcnt vmcnt(26) lgkmcnt(5)\nRA 113,113\nY 241,113,241\nM 0,204,136\n"
"s_waitcnt vmcnt(25)\nRA 113,114\nY 242,113,242\nM 4,192,136\ns_waitcnt vmcnt(24)\nRA 113,115\nY 243,113,243\nM 4,212,128\ns_waitcnt vmcnt(23)\nD 128,1,20000\nRA 113,116\n"
"M 4,216,132\nK 172,1024\nY 244,113,244\nM 8,200,132\nRA 113,117\nY 245,113,245\nM 8,196,136\ns_waitcnt lgkmcnt(5)\nRA 113,118\nY 246,113,246\nM 8,204,140\nRA 113,119\nY 247,113,247\n"
"M 12,192,140\nK 160,2048\nv_add_u32 v15, 0x2000, v15\nM 12,212,132\nD 132,1,20256\nRA 113,120\nM 12,216,136\nY 248,113,248\nRA 113,121\nM 16,200,136\nY 249,113,249\nRA 113,122\n"
"M 16,196,140\ns_waitcnt lgkmcnt(5)\nK 180,-4096\nY 250,113,250\nM 16,204,144\nRA 113,123\nY 251,113,251\nM 20,192,144\nRA 113,124\nY 252,113,252\nM 20,212,136\nD 136,1,17984\n"
"RA 113,125\nM 20,216,140\nK 184,-3072\nY 253,113,253\nM 24,200,140\nRA 113,126\nY 254,113,254\nM 24,196,144\ns_waitcnt lgkmcnt(5)\nRA 113,127\nY 255,113,255\nM 24,204,148\nL 82,41\n"
"L 90,45\nM 28,192,148\nK 188,-2048\nX 108,98,106\nM 28,212,140\nD 140,1,18240\nX 109,107,106\nM 28,216,144\nY 110,104,82\nY 98,105,90\nM 32,200,144\nY 111,110,107\nY 112,110,98\n"
"M 32,196,148\ns_waitcnt lgkmcnt(5)\nK 176,-1024\nA 114,33\nM 32,204,152\nA 115,37\nY 116,114,83\nM 36,192,152\nY 117,91,115\nA 83,41\nM 36,212,144\nD 144,1,18496\nA 91,45\n"
"M 36,216,148\nX 118,99,116\nX 119,117,116\nM 40,200,148\nY 120,114,83\nY 99,115,91\nM 40,196,152\ns_waitcnt lgkmcnt(5)\nY 121,120,117\nY 122,120,99\nM 40,204,156\nC 65,108,118\n"
"C 69,109,119\nM 44,192,156\nC 73,111,121\nC 77,112,122\nM 44,212,148\nD 148,1,18752\nL 104,34\nM 44,216,152\nL 105,38\nY 106,104,84\nM 48,200,152\nY 107,92,105\nL 84,42\n"
"M 48,196,156\ns_waitcnt lgkmcnt(5)\nL 92,46\nX 108,100,106\nM 48,204,128\nX 109,107,106\nY 110,104,84\nM 52,192,128\nY 100,105,92\nY 111,110,107\nM 52,212,152\nD 152,1,19008\n"
"Y 112,110,100\nM 52,216,156\nA 114,34\nA 115,38\nM 56,200,156\nK 200,3072\nY 116,114,85\nM 56,196,128\ns_waitcnt lgkmcnt(5)\nY 117,93,115\nA 85,42\nM 56,204,132\nA 93,46\n"
"X 118,101,116\nM 60,192,132\n.Lpad11:\n.if (.Lpad11 - .Lbase) != 45056\n.error \"pad\"\n.endif\ns_cbranch_execz .Lpad15\nD 132,1,19264\nX 119,117,116\nM 60,212,156\nD 156,1,19520\n"
"Y 120,114,85\nM 60,216,128\ns_waitcnt vmcnt(29) lgkmcnt(6)\nD 128,1,19808\nY 101,115,93\nM 0,220,136\ns_waitcnt vmcnt(17) lgkmcnt(6)\nK 196,0\nY 121,120,117\nM 0,208,140\n"
"s_waitcnt vmcnt(17) lgkmcnt(5)\nY 122,120,101\nC 66,108,118\nM 0,232,144\ns_waitcnt vmcnt(16)\nC 70,109,119\nC 74,111,121\nM 4,228,144\ns_waitcnt vmcnt(15)\nC 78,112,122\nL 104,35\n"
"M 4,236,136\ns_waitcnt vmcnt(14)\nD 136,1,20064\nL 105,39\nM 4,224,140\nK 204,1024\nY 106,104,86\nM 8,220,140\nY 107,94,105\nL 86,43\nM 8,208,144\ns_waitcnt lgkmcnt(5)\nL 94,47\n"
"X 108,102,106\nM 8,232,148\nX 109,107,106\nY 110,104,86\nM 12,228,148\nK 192,2048\nv_add_u32 v15, 0x1e000, v15\nM 12,236,140\nD 140,1,20320\nY 102,105,94\nM 12,224,144\n"
"Y 111,110,107\nY 112,110,102\nM 16,220,144\nA 114,35\nA 115,39\nM 16,208,148\ns_waitcnt lgkmcnt(5)\nG 32,8,3328\nM 16,232,152\nY 116,114,87\nY 117,95,115\nM 20,228,152\nA 87,43\n"
"A 95,47\nM 20,236,144\nD 144,1,35328\nX 118,103,116\nM 20,224,148\nG 36,9,256\nM 24,220,148\nX 119,117,116\nY 120,114,87\nM 24,208,152\ns_waitcnt lgkmcnt(5)\nY 103,115,95\n"
"Y 121,120,117\nM 24,232,132\nY 122,120,103\nC 67,108,118\nM 28,228,132\nG 40,9,1280\nM 28,236,148\nD 148,1,35584\nC 71,109,119\nM 28,224,152\nC 75,111,121\nC 79,112,122\n"
"M 32,220,152\nS 3,64,512\nM 32,208,132\ns_waitcnt lgkmcnt(6)\nG 44,9,2304\nM 32,232,156\nL 104,48\nL 105,52\nM 36,228,156\nY 106,104,80\nY 107,88,105\nM 36,236,152\nD 152,1,35840\n"
"L 80,56\nM 36,224,132\nK 212,-4096\nL 88,60\nM 40,220,132\nS 3,68,35840\nM 40,208,156\ns_waitcnt lgkmcnt(7)\nX 108,96,106\nX 109,107,106\nM 40,232,128\nY 110,104,80\nY 96,105,88\n"
"M 44,228,128\nK 216,-3072\nY 111,110,107\nM 44,236,132\nD 132,1,36096\nY 112,110,96\nM 44,224,156\nS 5,72,18176\nM 48,220,156\nA 114,48\nA 115,52\nM 48,208,128\n"
"s_waitcnt lgkmcnt(8)\nY 116,114,81\nY 117,89,115\nM 48,232,136\nA 81,56\nA 89,60\nM 52,228,136\nX 118,97,116\nX 119,117,116\nM 52,236,156\nD 156,1,36352\nY 120,114,81\nM 52,224,128\n"
"S 3,76,18176\nM 56,220,128\nK 220,-2048\nY 97,115,89\nM 56,208,136\ns_waitcnt lgkmcnt(9)\nY 121,120,117\nY 122,120,97\nM 56,232,140\nC 64,108,118\nC 68,109,119\nM 60,228,140\n"
"D 140,1,36608\nC 72,111,121\nM 60,236,128\nD 128,1,36864\nC 76,112,122\nM 60,224,136\ns_waitcnt vmcnt(22) lgkmcnt(10)\nD 136,1,37120\nL 104,49\nN 128,244,144\n"
"s_waitcnt vmcnt(21) lgkmcnt(10)\nK 208,-1024\nL 105,53\nN 128,248,148\ns_waitcnt vmcnt(21) lgkmcnt(8)\nY 106,104,82\nY 107,90,105\nN 128,252,152\ns_waitcnt vmcnt(20) lgkmcnt(6)\n"
"L 82,57\nL 90,61\nN 128,240,132\ns_waitcnt vmcnt(19)\nX 108,98,106\nX 109,107,106\nN 132,168,132\ns_waitcnt vmcnt(18)\nK 232,3072\nY 110,104,82\nN 132,164,144\ns_waitcnt vmcnt(18)\n"
"D 144,1,37408\nY 98,105,90\nN 132,172,148\ns_waitcnt vmcnt(17)\nY 111,110,107\nY 112,110,98\nN 132,160,152\nA 114,49\nA 115,53\nN 136,248,152\nK 228,0\nY 116,114,83\nN 136,252,132\n"
"s_waitcnt lgkmcnt(5)\nY 117,91,115\nA 83,57\nN 136,240,156\nA 91,61\nX 118,99,116\nN 136,244,148\nX 119,117,116\nY 120,114,83\nN 140,164,148\nD 148,1,37664\nY 99,115,91\n"
"N 140,172,152\nK 236,1024\nY 121,120,117\nN 140,160,132\nY 122,120,99\nC 65,108,118\nN 140,168,156\nC 69,109,119\nC 73,111,121\nN 144,252,156\nC 77,112,122\nL 104,50\nN 144,248,132\n"
"s_waitcnt lgkmcnt(4)\nK 224,2048\nv_add_u32 v15, 0x2000, v15\nN 144,240,140\nL 105,54\nY 106,104,84\nN 144,244,152\nY 107,92,105\nL 84,58\nN 148,164,152\nD 152,1,37920\nL 92,62\n"
"N 148,172,132\nX 108,100,106\nX 109,107,106\nN 148,160,156\nY 110,104,84\nY 100,105,92\nN 148,168,140\nY 111,110,107\nY 112,110,100\nN 152,252,140\nA 114,50\nA 115,54\n"
"N 152,248,156\ns_waitcnt lgkmcnt(4)\nY 116,114,85\nY 117,93,115\nN 152,240,128\nA 85,58\nA 93,62\nN 152,244,132\nX 118,101,116\nX 119,117,116\nN 156,164,132\nD 132,1,35392\n"
"Y 120,114,85\nN 156,172,156\nY 101,115,93\nY 121,120,117\nN 156,160,140\nY 122,120,101\nC 66,108,118\nN 156,168,128\nC 70,109,119\nC 74,111,121\nN 160,252,128\nC 78,112,122\n"
"L 104,51\nN 160,248,140\ns_waitcnt lgkmcnt(4)\nL 105,55\nY 106,104,86\nN 160,240,136\nY 107,94,105\nL 86,59\nN 160,244,156\nL 94,63\nX 108,102,106\nN 164,164,156\nD 156,1,35648\n"
"X 109,107,106\nN 164,172,140\nY 110,104,86\nY 102,105,94\nN 164,160,128\nY 111,110,107\nY 112,110,102\nN 164,168,136\nA 114,51\nA 115,55\nN 168,252,136\nG 48,9,3328\nN 168,248,128\n"
"s_waitcnt lgkmcnt(4)\nY 116,114,87\nY 117,95,115\nN 168,240,144\nA 87,59\nA 95,63\nN 168,244,140\nX 118,103,116\nX 119,117,116\nN 172,164,140\nD 140,1,35904\nY 120,114,87\n"
"N 172,172,128\nG 52,10,256\nN 172,160,136\nY 103,115,95\nY 121,120,117\nN 172,168,144\nY 122,120,103\nC 67,108,118\nN 176,252,144\nC 71,109,119\nC 75,111,121\nN 176,248,136\n"
"s_waitcnt lgkmcnt(4)\nG 56,10,1280\nN 176,240,148\nC 79,112,122\nL 80,17\nN 176,244,128\nL 81,18\nL 82,19\nN 180,164,128\nD 128,1,36160\nY 80,80,82\nN 180,172,136\nG 60,10,2304\n"
"N 180,160,144\nS 3,64,768\nN 180,168,148\nL 104,20\nL 105,21\nN 184,252,148\nY 106,104,81\nY 107,82,105\nN 184,248,144\ns_waitcnt lgkmcnt(5)\nK 252,-4096\nL 81,22\nN 184,240,152\n"
"S 3,68,36096\nN 184,244,136\nL 82,23\nX 108,80,106\nN 188,164,136\nX 109,107,106\nY 110,104,81\nN 188,172,144\nD 136,1,36416\nY 80,105,82\nN 188,160,148\nD 144,1,36672\n"
"Y 111,110,107\nN 188,168,152\ns_waitcnt vmcnt(24) lgkmcnt(7)\nD 148,1,36928\nY 112,110,80\nN 128,180,132\ns_waitcnt vmcnt(23) lgkmcnt(7)\nD 152,1,37184\nA 84,17\nN 128,184,156\n"
"s_waitcnt vmcnt(22) lgkmcnt(7)\nK 248,-3072\nA 85,18\nN 128,188,140\ns_waitcnt vmcnt(22) lgkmcnt(6)\nS 5,72,18432\nN 128,176,128\ns_waitcnt vmcnt(21)\nA 86,19\nY 84,84,86\n"
"N 132,200,128\ns_waitcnt vmcnt(20)\nA 114,20\nA 115,21\nN 132,196,132\ns_waitcnt vmcnt(19)\nD 132,1,37472\nY 116,114,85\nN 132,204,156\ns_waitcnt vmcnt(18)\nK 240,-2048\n"
"Y 117,86,115\nN 132,192,140\nS 3,76,18432\nN 136,184,140\nA 85,22\nA 86,23\nN 136,188,128\ns_waitcnt lgkmcnt(6)\nX 118,84,116\nX 119,117,116\nN 136,176,136\nK 244,-1024\n"
"Y 120,114,85\nN 136,180,156\nY 84,115,86\nY 121,120,117\nN 140,196,156\nD 156,1,37728\nY 122,120,84\nN 140,204,140\nC 64,108,118\nC 65,109,119\nN 140,192,128\nK 164,3072\n"
"C 66,111,121\nN 140,200,136\nC 67,112,122\nL 104,24\nN 144,188,136\nU 6,64,0\nN 144,184,128\ns_waitcnt lgkmcnt(7)\nU 6,65,35328\nN 144,176,144\nK 172,0\nL 80,28\nN 144,180,140\n"
"U 7,66,52992\nN 148,196,140\ns_waitcnt vmcnt(21)\nD 140,1,37984\nL 88,32\nN 148,204,128\nU 6,67,17664\nN 148,192,136\nK 160,1024\nY 96,104,88\nN 148,200,144\nA 105,24\nA 81,28\n"
"N 152,188,144\nA 89,32\nY 97,105,89\nN 152,184,136\ns_waitcnt lgkmcnt(0)\ns_barrier\nL 104,25\nN 152,176,148\nK 168,2048\nv_add_u32 v15, 0xfffee000, v15\nN 152,180,128\nL 82,29\n"
"L 90,33\nN 156,196,128\nD 128,2,0\nY 98,104,90\nN 156,204,136\nA 105,25\nA 83,29\nN 156,192,144\nA 91,33\nY 99,105,91\nN 156,200,148\nL 104,26\nL 84,30\nN 160,188,148\nL 92,34\n"
"Y 100,104,92\nN 160,184,144\nA 105,26\nA 85,30\nN 160,176,152\nA 93,34\nY 101,105,93\nN 160,180,136\nL 104,27\nL 86,31\nN 164,196,136\nD 136,2,256\nL 94,35\nN 164,204,144\n"
"Y 102,104,94\nA 105,27\nN 164,192,148\nG 24,10,3328\nN 164,200,152\nA 87,31\nA 95,35\nN 168,188,152\ns_waitcnt vmcnt(23)\nY 103,105,95\nL 104,36\nN 168,184,148\ns_waitcnt vmcnt(22)\n"
"L 105,40\nY 106,104,80\nN 168,176,132\nG 28,11,256\nN 168,180,144\ns_waitcnt vmcnt(22)\nY 107,88,105\nL 80,44\nN 172,196,144\ns_waitcnt vmcnt(13)\nD 144,2,512\nL 88,48\n"
"N 172,204,148\nX 108,96,106\nX 109,107,106\nN 172,192,152\nG 32,11,1280\nN 172,200,132\nY 110,104,80\nY 96,105,88\nN 176,188,132\nY 111,110,107\nY 112,110,96\nN 176,184,152\n"
"A 114,36\nA 115,40\nN 176,176,156\nY 116,114,81\nY 117,89,115\nN 176,180,148\nA 81,44\nA 89,48\nN 180,196,148\nD 148,2,768\nX 118,97,116\nN 180,204,152\nX 119,117,116\nY 120,114,81\n"
"N 180,192,132\nY 97,115,89\nY 121,120,117\nN 180,200,156\nY 122,120,97\nC 64,108,118\nN 184,188,156\nK 188,-4096\nC 68,109,119\nN 184,184,132\nC 124,111,121\nC 76,112,122\n"
"N 184,176,140\nL 104,37\nL 105,41\nN 184,180,152\nY 106,104,82\nY 107,90,105\nN 188,196,152\nD 152,2,1024\nL 82,45\nN 188,204,132\nD 132,2,1280\nL 90,49\nN 188,192,156\n"
"D 156,2,1536\nX 108,98,106\nN 188,200,140\ns_waitcnt lgkmcnt(6)\nD 140,2,1792\nX 109,107,106\nM 64,212,128\ns_waitcnt lgkmcnt(6)\nK 184,-3072\nY 110,104,82\nM 64,216,136\n"
"s_waitcnt lgkmcnt(5)\nY 98,105,90\nY 111,110,107\nM 64,220,144\ns_waitcnt lgkmcnt(4)\nY 112,110,98\nA 114,37\nM 64,208,148\nA 115,41\nY 116,114,83\nM 68,232,148\nK 176,-2048\n"
"Y 117,91,115\nM 68,228,128\nD 128,2,2080\nA 83,45\nM 68,236,136\nA 91,49\nX 118,99,116\nM 68,224,144\nX 119,117,116\nY 120,114,83\nM 72,216,144\nK 180,-1024\nY 99,115,91\n"
"M 72,220,148\ns_waitcnt lgkmcnt(4)\nY 121,120,117\nY 122,120,99\nM 72,208,152\nC 65,108,118\nC 69,109,119\nM 72,212,136\nC 125,111,121\nC 77,112,122\nM 76,228,136\nD 136,2,2336\n"
"L 104,38\nM 76,236,144\nK 196,3072\nL 105,42\nM 76,224,148\nY 106,104,84\nY 107,92,105\nM 76,232,152\nL 84,46\nL 92,50\nM 80,220,152\nX 108,100,106\nX 109,107,106\nM 80,216,148\n"
"s_waitcnt lgkmcnt(4)\nK 204,0\nY 110,104,84\nM 80,208,132\nY 100,105,92\nY 111,110,107\nM 80,212,144\nY 112,110,100\nA 114,38\nM 84,228,144\nD 144,2,2592\nA 115,42\nM 84,236,148\n"
"K 192,1024\nY 116,114,85\nM 84,224,152\nY 117,93,115\nA 85,46\nM 84,232,132\nA 93,50\nX 118,101,116\nM 88,220,132\nX 119,117,116\nY 120,114,85\nM 88,216,152\ns_waitcnt lgkmcnt(4)\n"
"K 200,2048\nv_add_u32 v15, 0x2000, v15\nM 88,208,156\nY 101,115,93\nY 121,120,117\nM 88,212,148\nY 122,120,101\nC 66,108,118\nM 92,228,148\nD 148,2,64\nC 70,109,119\nM 92,236,152\n"
"C 126,111,121\nC 78,112,122\nM 92,224,132\nL 104,39\nL 105,43\nM 92,232,156\nY 106,104,86\nY 107,94,105\nM 96,220,156\nL 86,47\nL 94,51\nM 96,216,132\ns_waitcnt lgkmcnt(4)\n"
"X 108,102,106\nX 109,107,106\nM 96,208,140\nY 110,104,86\nY 102,105,94\nM 96,212,152\nY 111,110,107\nY 112,110,102\nM 100,228,152\nD 152,2,320\nA 114,39\nM 100,236,132\n.Lpad12:\n"
".if (.Lpad12 - .Lbase) != 49168\n.error \"pad\"\n.endif\ns_cbranch_execz .Lret1\nG 36,11,2304\nM 100,224,156\nA 115,43\nY 116,114,87\nM 100,232,140\nY 117,95,115\nA 87,47\n"
"M 104,220,140\nA 95,51\nX 118,103,116\nM 104,216,156\ns_waitcnt lgkmcnt(4)\nG 40,11,3328\nM 104,208,128\nX 119,117,116\nY 120,114,87\nM 104,212,132\nY 103,115,95\nY 121,120,117\n"
"M 108,228,132\nD 132,2,576\nY 122,120,103\nM 108,236,156\nG 44,12,256\nM 108,224,140\nC 67,108,118\nC 71,109,119\nM 108,232,128\nC 127,111,121\nC 79,112,122\nM 112,220,128\n"
"S 4,64,35328\nM 112,216,140\ns_waitcnt lgkmcnt(5)\nG 48,12,1280\nM 112,208,136\ns_waitcnt vmcnt(24)\nL 104,52\nL 105,56\nM 112,212,156\nY 106,104,80\nY 107,88,105\nM 116,228,156\n"
"s_waitcnt vmcnt(23)\nD 156,2,832\nL 80,60\nM 116,236,140\ns_waitcnt vmcnt(14)\nL 88,24\nX 108,96,106\nM 116,224,128\nX 109,107,106\nY 110,104,80\nM 116,232,136\nS 4,68,17664\n"
"M 120,220,136\nK 220,-4096\nY 96,105,88\nM 120,216,128\ns_waitcnt lgkmcnt(6)\nY 111,110,107\nY 112,110,96\nM 120,208,144\nA 114,52\nA 115,56\nM 120,212,140\nY 116,114,81\n"
"Y 117,89,115\nM 124,228,140\nD 140,2,1088\nA 81,60\nM 124,236,128\nD 128,2,1344\nA 89,24\nM 124,224,136\nD 136,2,1600\nX 118,97,116\nM 124,232,144\ns_waitcnt lgkmcnt(8)\n"
"D 144,2,1856\nX 119,117,116\nM 64,252,148\ns_waitcnt lgkmcnt(8)\nK 216,-3072\nY 120,114,81\nM 64,248,152\ns_waitcnt lgkmcnt(7)\nS 4,76,0\nM 64,240,132\ns_waitcnt lgkmcnt(6)\n"
"Y 97,115,89\nY 121,120,117\nM 64,244,156\nY 122,120,97\nC 64,108,118\nM 68,164,156\nK 208,-2048\nC 68,109,119\nM 68,172,148\nD 148,2,2144\nC 72,111,121\nM 68,160,152\nC 76,112,122\n"
"L 104,53\nM 68,168,132\nL 105,57\nY 106,104,82\nM 72,248,132\nK 212,-1024\nY 107,90,105\nM 72,240,156\ns_waitcnt lgkmcnt(5)\nL 82,61\nL 90,25\nM 72,244,140\nX 108,98,106\n"
"X 109,107,106\nM 72,252,152\nY 110,104,82\nY 98,105,90\nM 76,172,152\nD 152,2,2400\nY 111,110,107\nM 76,160,132\nK 228,3072\nY 112,110,98\nM 76,168,156\nA 114,53\nA 115,57\n"
"M 76,164,140\nY 116,114,83\nY 117,91,115\nM 80,240,140\nA 83,61\nA 91,25\nM 80,248,156\ns_waitcnt lgkmcnt(5)\nK 236,0\nX 118,99,116\nM 80,244,128\nX 119,117,116\nY 120,114,83\n"
"M 80,252,132\nY 99,115,91\nY 121,120,117\nM 84,172,132\nD 132,2,2656\nY 122,120,99\nM 84,160,156\nK 224,1024\nC 65,108,118\nM 84,168,140\nC 69,109,119\nC 73,111,121\nM 84,164,128\n"
"C 77,112,122\nL 104,54\nM 88,240,128\nL 105,58\nY 106,104,84\nM 88,248,140\ns_waitcnt lgkmcnt(0)\ns_barrier\nY 107,92,105\nM 88,244,136\nK 232,2048\nv_add_u32 v15, 0xfffe2000, v15\n"
"M 88,252,156\nL 84,62\nL 92,26\nM 92,172,156\nD 156,2,35328\nX 108,100,106\nM 92,160,140\nX 109,107,106\nY 110,104,84\nM 92,168,128\nY 100,105,92\nY 111,110,107\nM 92,164,136\n"
"Y 112,110,100\nA 114,54\nM 96,240,136\nA 115,58\nY 116,114,85\nM 96,248,128\nY 117,93,115\nA 85,62\nM 96,244,144\nA 93,26\nX 118,101,116\nM 96,252,140\nX 119,117,116\nY 120,114,85\n"
"M 100,172,140\nD 140,2,35584\nY 101,115,93\nM 100,160,128\nY 121,120,117\nY 122,120,101\nM 100,168,136\nC 66,108,118\nC 70,109,119\nM 100,164,144\nC 74,111,121\nC 78,112,122\n"
"M 104,240,144\nL 104,55\nL 105,59\nM 104,248,136\nY 106,104,86\nY 107,94,105\nM 104,244,148\nL 86,63\nL 94,27\nM 104,252,128\nX 108,102,106\nX 109,107,106\nM 108,172,128\n"
"D 128,2,35840\nY 110,104,86\nM 108,160,136\nY 102,105,94\nY 111,110,107\nM 108,168,144\nY 112,110,102\nA 114,55\nM 108,164,148\nG 52,12,2304\nM 112,240,148\nA 115,59\nY 116,114,87\n"
"M 112,248,144\nY 117,95,115\nA 87,63\nM 112,244,152\nA 95,27\nX 118,103,116\nM 112,252,136\nG 56,12,3328\nM 116,172,136\nD 136,2,36096\nX 119,117,116\nM 116,160,144\nY 120,114,87\n"
"Y 103,115,95\nM 116,168,148\nY 121,120,117\nY 122,120,103\nM 116,164,152\nH 17,13,256\nM 120,240,152\nC 67,108,118\nC 71,109,119\nM 120,248,148\nH 18,13,1280\nM 120,244,132\n"
"C 75,111,121\nC 79,112,122\nM 120,252,144\nH 19,13,2304\nM 124,172,144\ns_waitcnt vmcnt(26)\nD 144,2,36352\nL 104,28\nM 124,160,148\ns_waitcnt vmcnt(25)\nD 148,2,36608\nL 105,32\n"
"M 124,168,152\nD 152,2,36864\nY 106,104,80\nM 124,164,132\ns_waitcnt vmcnt(24) lgkmcnt(6)\nD 132,2,37120\nY 107,88,105\nN 192,188,156\ns_waitcnt vmcnt(23) lgkmcnt(6)\nH 20,13,3328\n"
"N 192,184,140\ns_waitcnt vmcnt(23) lgkmcnt(5)\nS 4,64,35584\nN 192,176,128\ns_waitcnt vmcnt(22) lgkmcnt(5)\nH 21,14,256\nN 192,180,136\ns_waitcnt vmcnt(17)\nL 80,36\nL 88,40\n"
"N 196,196,136\nH 22,14,1280\nN 196,204,156\nD 156,2,37408\nX 108,96,106\nN 196,192,140\nH 23,14,2304\nN 196,200,128\nS 4,68,17920\nN 200,184,128\nG 60,8,1408\nN 200,176,136\n"
"s_waitcnt lgkmcnt(6)\nX 109,107,106\nY 110,104,80\nN 200,180,144\nS 4,76,256\nN 200,188,140\nY 96,105,88\nY 111,110,107\nN 204,204,140\nG 24,8,2432\nN 204,192,128\nD 140,2,37664\n"
"Y 112,110,96\nN 204,200,136\nS 5,72,256\nN 204,196,144\nA 114,28\nA 115,32\nN 208,176,144\nK 240,-3072\nY 116,114,81\nN 208,184,136\ns_waitcnt lgkmcnt(8)\nY 117,89,115\nA 81,36\n"
"N 208,180,148\nS 5,124,0\nN 208,188,128\nA 89,40\nX 118,97,116\nN 212,204,128\nK 248,-2048\nX 119,117,116\nN 212,192,136\nD 128,2,37920\nY 120,114,81\nN 212,200,144\nY 97,115,89\n"
"Y 121,120,117\nN 212,196,148\nY 122,120,97\nC 64,108,118\nN 216,176,148\nK 244,-1024\nC 68,109,119\nN 216,184,144\ns_waitcnt lgkmcnt(9)\nC 72,111,121\nC 76,112,122\nN 216,180,152\n"
"L 104,29\nL 105,33\nN 216,188,136\nY 106,104,82\nY 107,90,105\nN 220,204,136\nD 136,2,35392\nL 82,37\nN 220,192,144\nK 252,3072\nL 90,41\nN 220,200,148\nX 108,98,106\nX 109,107,106\n"
"N 220,196,152\nY 110,104,82\nY 98,105,90\nN 224,176,152\nY 111,110,107\nY 112,110,98\nN 224,184,148\ns_waitcnt lgkmcnt(9)\nK 172,1024\nA 114,29\nN 224,180,132\nA 115,33\n"
"Y 116,114,83\nN 224,188,144\nY 117,91,115\nA 83,37\nN 228,204,144\nD 144,2,35648\nA 91,41\nN 228,192,148\nK 160,2048\nv_add_u32 v15, 0x2000, v15\nN 228,200,152\nX 118,99,116\n"
"X 119,117,116\nN 228,196,132\nY 120,114,83\nY 99,115,91\nN 232,176,132\nY 121,120,117\nY 122,120,99\nN 232,184,152\ns_waitcnt lgkmcnt(8)\nK 168,-3072\nC 65,108,118\nN 232,180,156\n"
"C 69,109,119\nC 73,111,121\nN 232,188,148\nC 77,112,122\nL 104,30\nN 236,204,148\nD 148,2,35904\nL 105,34\nN 236,192,152\nK 164,-2048\nY 106,104,84\nN 236,200,132\nY 107,92,105\n"
"L 84,38\nN 236,196,156\nL 92,42\nX 108,100,106\nN 240,176,156\nX 109,107,106\nY 110,104,84\nN 240,184,132\ns_waitcnt lgkmcnt(6)\nY 100,105,92\nY 111,110,107\nN 240,180,140\n"
"Y 112,110,100\nA 114,30\nN 240,188,152\nA 115,34\nY 116,114,85\nN 244,204,152\nD 152,2,36160\nY 117,93,115\nN 244,192,132\nA 85,38\nA 93,42\nN 244,200,156\nX 118,101,116\n"
"X 119,117,116\nN 244,196,140\nY 120,114,85\nY 101,115,93\nN 248,176,140\nK 176,-1024\nY 121,120,117\nN 248,184,156\ns_waitcnt lgkmcnt(4)\nY 122,120,101\nC 66,108,118\nN 248,180,128\n"
"C 70,109,119\nC 74,111,121\nN 248,188,132\nC 78,112,122\nL 104,31\nN 252,204,132\nD 132,2,36416\nL 105,35\nN 252,192,156\nD 156,2,36672\nY 106,104,86\nN 252,200,140\nD 140,2,36928\n"
"Y 107,94,105\nN 252,196,128\ns_waitcnt vmcnt(27) lgkmcnt(6)\nD 128,2,37184\nL 86,39\nN 192,220,136\ns_waitcnt vmcnt(26) lgkmcnt(6)\nK 184,3072\nL 94,43\nN 192,216,144\n"
"s_waitcnt vmcnt(26) lgkmcnt(5)\nX 108,102,106\nX 109,107,106\nN 192,208,148\ns_waitcnt vmcnt(25) lgkmcnt(4)\nY 110,104,86\nY 102,105,94\nN 192,212,152\ns_waitcnt vmcnt(24)\n"
"Y 111,110,107\nY 112,110,102\nN 196,228,152\ns_waitcnt vmcnt(23)\nK 180,1024\nA 114,31\nN 196,236,136\ns_waitcnt vmcnt(23)\nD 136,2,37472\nA 115,35\nN 196,224,144\n"
"s_waitcnt vmcnt(22)\nY 116,114,87\nY 117,95,115\nN 196,232,148\nA 87,39\nA 95,43\nN 200,216,148\nK 188,2048\nv_add_u32 v15, 0x2e000, v15\nN 200,208,152\ns_waitcnt lgkmcnt(4)\n"
"X 118,103,116\nX 119,117,116\nN 200,212,132\nY 120,114,87\nY 103,115,95\nN 200,220,144\nY 121,120,117\nY 122,120,103\nN 204,236,144\nD 144,2,37728\nC 67,108,118\nN 204,224,148\n"
"G 28,8,3456\nN 204,232,152\nC 71,109,119\nC 75,111,121\nN 204,228,132\nC 79,112,122\nL 104,44\nN 208,208,132\nS 4,64,35840\nN 208,216,152\ns_waitcnt lgkmcnt(5)\nG 32,9,384\n"
"N 208,212,156\nL 105,48\nY 106,104,80\nN 208,220,148\ns_waitcnt vmcnt(24)\nY 107,88,105\nL 80,52\nN 212,236,148\ns_waitcnt vmcnt(23)\nD 148,2,37984\nL 88,56\nN 212,224,152\n"
"G 36,9,1408\nN 212,232,132\nS 4,68,18176\nN 212,228,156\nX 108,96,106\nX 109,107,106\nN 216,208,156\nY 110,104,80\nY 96,105,88\nN 216,216,132\ns_waitcnt lgkmcnt(6)\nG 40,9,2432\n"
"N 216,212,140\nY 111,110,107\nY 112,110,96\nN 216,220,152\nA 114,44\nA 115,48\nN 220,236,152\nD 152,0,256\nY 116,114,81\nN 220,224,132\nK 204,-4096\nY 117,89,115\nN 220,232,156\n"
"S 5,72,512\nN 220,228,140\nA 81,52\nA 89,56\nN 224,208,140\nX 118,97,116\nX 119,117,116\nN 224,216,156\ns_waitcnt lgkmcnt(7)\nK 192,-3072\nY 120,114,81\nN 224,212,128\nS 4,76,512\n"
"N 224,220,132\nY 97,115,89\nY 121,120,117\nN 228,236,132\nY 122,120,97\nC 64,108,118\nN 228,224,156\nD 132,0,512\nC 68,109,119\nN 228,232,140\nK 200,-2048\nC 72,111,121\n"
"N 228,228,128\nC 76,112,122\nL 104,45\nN 232,208,128\nL 105,49\nY 106,104,82\nN 232,216,140\ns_waitcnt lgkmcnt(8)\nY 107,90,105\nL 82,53\nN 232,212,136\nK 196,-1024\nL 90,57\n"
"N 232,220,156\nX 108,98,106\nX 109,107,106\nN 236,236,156\nD 156,0,768\nY 110,104,82\nN 236,224,140\nY 98,105,90\nY 111,110,107\nN 236,232,128\nY 112,110,98\nA 114,45\n"
"N 236,228,136\nA 115,49\nY 116,114,83\nN 240,208,136\nY 117,91,115\nA 83,53\nN 240,216,128\ns_waitcnt lgkmcnt(8)\nA 91,57\nX 118,99,116\nN 240,212,144\nX 119,117,116\nY 120,114,83\n"
"N 240,220,140\nY 99,115,91\nY 121,120,117\nN 244,236,140\nD 140,0,1024\nY 122,120,99\nN 244,224,128\nC 65,108,118\nC 69,109,119\nN 244,232,136\nC 73,111,121\nC 77,112,122\n"
"N 244,228,144\nL 104,46\nL 105,50\nN 248,208,144\nK 208,3072\nY 106,104,84\nN 248,216,136\ns_waitcnt lgkmcnt(7)\nY 107,92,105\nL 84,54\nN 248,212,148\nL 92,58\nX 108,100,106\n"
"N 248,220,128\nX 109,107,106\nY 110,104,84\nN 252,236,128\nD 128,0,1280\nY 100,105,92\nN 252,224,136\nD 136,0,1536\nY 111,110,107\nN 252,232,144\n.Lpad13:\n"
".if (.Lpad13 - .Lbase) != 53256\n.error \"pad\"\n.endif\ns_cbranch_execz .Lret1\nD 144,0,1792\nY 112,110,100\nN 252,228,148\ns_waitcnt vmcnt(20) lgkmcnt(8)\nD 148,0,2080\nA 114,46\n"
"M 0,240,152\ns_waitcnt vmcnt(19) lgkmcnt(6)\nK 216,0\nA 115,50\nM 0,248,132\ns_waitcnt vmcnt(19) lgkmcnt(5)\nY 116,114,85\nY 117,93,115\nM 0,244,156\ns_waitcnt vmcnt(18)\nA 85,54\n"
"A 93,58\nM 4,252,156\ns_waitcnt vmcnt(17)\nX 118,101,116\nX 119,117,116\nM 4,172,152\ns_waitcnt vmcnt(16)\nD 152,0,2336\nY 120,114,85\nM 4,160,132\nK 212,1024\nY 101,115,93\n"
"M 8,240,132\nY 121,120,117\nY 122,120,101\nM 8,248,156\ns_waitcnt lgkmcnt(5)\nC 66,108,118\nC 70,109,119\nM 8,244,140\nC 74,111,121\nC 78,112,122\nM 12,252,140\nK 220,2048\n"
"v_add_u32 v15, 0x2000, v15\nM 12,172,132\nD 132,0,2592\nL 104,47\nM 12,160,156\nL 105,51\nY 106,104,86\nM 16,240,156\nY 107,94,105\nL 86,55\nM 16,248,140\ns_waitcnt lgkmcnt(5)\n"
"K 236,-4096\nL 94,59\nM 16,244,128\nX 108,102,106\nX 109,107,106\nM 20,252,128\nY 110,104,86\nY 102,105,94\nM 20,172,156\nD 156,0,320\nY 111,110,107\nM 20,160,140\nK 224,-3072\n"
"Y 112,110,102\nM 24,240,140\nA 114,47\nA 115,51\nM 24,248,128\ns_waitcnt lgkmcnt(5)\nY 116,114,87\nY 117,95,115\nM 24,244,136\nA 87,55\nA 95,59\nM 28,252,136\nG 44,9,3456\n"
"M 28,172,140\nD 140,0,576\nX 118,103,116\nM 28,160,128\nX 119,117,116\nY 120,114,87\nM 32,240,128\nY 103,115,95\nY 121,120,117\nM 32,248,136\ns_waitcnt lgkmcnt(5)\nG 48,10,384\n"
"M 32,244,144\nY 122,120,103\nC 67,108,118\nM 36,252,144\nC 71,109,119\nC 75,111,121\nM 36,172,128\nD 128,0,832\nC 79,112,122\nM 36,160,136\nG 52,10,1408\nM 40,240,136\nS 4,64,36096\n"
"M 40,248,144\ns_waitcnt lgkmcnt(6)\nL 80,17\nL 81,18\nM 40,244,148\nL 82,19\nY 80,80,82\nM 44,252,148\nG 56,10,2432\nM 44,172,136\nD 136,0,1088\nL 104,20\nM 44,160,144\nL 105,21\n"
"Y 106,104,81\nM 48,240,144\nY 107,82,105\nL 81,22\nM 48,248,148\ns_waitcnt lgkmcnt(6)\nK 232,-2048\nL 82,23\nM 48,244,152\nS 4,68,18432\nM 52,252,152\nX 108,80,106\nX 109,107,106\n"
"M 52,172,144\nY 110,104,81\nY 80,105,82\nM 52,160,148\nD 144,0,1344\nY 111,110,107\nM 56,240,148\nK 228,-1024\nY 112,110,80\nM 56,248,152\ns_waitcnt lgkmcnt(7)\nS 5,72,768\n"
"M 56,244,132\nA 84,17\nA 85,18\nM 60,252,132\nA 86,19\nY 84,84,86\nM 60,172,148\nD 132,0,1600\nA 114,20\nM 60,160,152\ns_waitcnt vmcnt(25) lgkmcnt(8)\nD 148,0,1856\nA 115,21\n"
"M 0,168,156\ns_waitcnt vmcnt(24) lgkmcnt(8)\nD 152,0,2144\nY 116,114,85\nM 0,164,140\ns_waitcnt vmcnt(23) lgkmcnt(8)\nK 240,3072\nY 117,86,115\nM 0,176,128\ns_waitcnt vmcnt(23)\n"
"S 4,76,768\nM 4,184,128\ns_waitcnt vmcnt(22)\nA 85,22\nA 86,23\nM 4,180,156\ns_waitcnt vmcnt(21)\nX 118,84,116\nX 119,117,116\nM 4,188,140\nD 156,0,2400\nY 120,114,85\nM 8,168,140\n"
"K 248,0\nY 84,115,86\nM 8,164,128\ns_waitcnt lgkmcnt(8)\nY 121,120,117\nY 122,120,84\nM 8,176,136\nC 64,108,118\nC 65,109,119\nM 12,184,136\nC 66,111,121\nC 67,112,122\n"
"M 12,180,140\nD 140,0,2656\nL 104,60\nM 12,188,128\nK 244,1024\nL 80,24\nM 16,168,128\nU 7,64,17664\nM 16,164,136\ns_waitcnt lgkmcnt(8)\nU 7,65,0\nM 16,176,144\nU 7,66,35328\n"
"M 20,184,144\nK 252,2048\nv_add_u32 v15, 0xfffd2000, v15\nM 20,180,128\ns_waitcnt vmcnt(23)\nD 128,0,17664\nL 88,28\nM 20,188,136\nU 6,67,52992\nM 24,168,136\nY 96,104,88\nA 105,60\n"
"M 24,164,144\ns_waitcnt lgkmcnt(10)\nK 172,-3072\nA 81,24\nM 24,176,132\nA 89,28\nY 97,105,89\nM 28,184,132\nL 104,61\nL 82,25\nM 28,180,136\nD 136,0,17920\nL 90,29\nM 28,188,144\n"
"K 160,-2048\nY 98,104,90\nM 32,168,144\nA 105,61\nA 83,25\nM 32,164,132\ns_waitcnt lgkmcnt(10)\nA 91,29\nY 99,105,91\nM 32,176,148\nL 104,62\nL 84,26\nM 36,184,148\nL 92,30\n"
"Y 100,104,92\nM 36,180,144\nD 144,0,18176\nA 105,62\nM 36,188,132\nA 85,26\nA 93,30\nM 40,168,132\nY 101,105,93\nL 104,63\nM 40,164,148\ns_waitcnt lgkmcnt(10)\nL 86,27\nL 94,31\n"
"M 40,176,152\nY 102,104,94\nA 105,63\nM 44,184,152\nG 60,10,3456\nM 44,180,132\nD 132,0,18432\nA 87,27\nM 44,188,148\nA 95,31\nY 103,105,95\nM 48,168,148\ns_waitcnt vmcnt(24)\n"
"L 104,32\nL 105,36\nM 48,164,152\ns_waitcnt lgkmcnt(9)\nG 24,11,384\nM 48,176,156\nY 106,104,80\nY 107,88,105\nM 52,184,156\ns_waitcnt vmcnt(13)\nL 80,40\nL 88,44\nM 52,180,148\n"
"D 148,0,18688\nX 108,96,106\nM 52,188,152\nG 28,11,1408\nM 56,168,152\nX 109,107,106\nY 110,104,80\nM 56,164,156\ns_waitcnt lgkmcnt(9)\nY 96,105,88\nY 111,110,107\nM 56,176,140\n"
"Y 112,110,96\nA 114,32\nM 60,184,140\nD 140,0,18944\nA 115,36\nM 60,180,152\nD 152,0,19200\nY 116,114,81\nM 60,188,156\ns_waitcnt lgkmcnt(7)\nD 156,0,19456\nY 117,89,115\n"
"M 64,204,128\ns_waitcnt lgkmcnt(6)\nK 168,-1024\nA 81,40\nM 64,192,136\ns_waitcnt lgkmcnt(5)\nA 89,44\nX 118,97,116\nM 64,200,144\ns_waitcnt lgkmcnt(4)\nX 119,117,116\nY 120,114,81\n"
"M 64,196,132\nY 97,115,89\nY 121,120,117\nM 68,208,132\nK 164,3072\nY 122,120,97\nM 68,216,128\nD 128,0,19744\nC 64,108,118\nM 68,212,136\nC 68,109,119\nC 72,111,121\nM 68,220,144\n"
"C 124,112,122\nL 104,33\nM 72,192,144\nK 176,1024\nL 105,37\nM 72,200,132\ns_waitcnt lgkmcnt(4)\nY 106,104,82\nY 107,90,105\nM 72,196,148\nL 82,41\nL 90,45\nM 72,204,136\n"
"X 108,98,106\nX 109,107,106\nM 76,216,136\nD 136,0,20000\nY 110,104,82\nM 76,212,144\nK 184,2048\nv_add_u32 v15, 0x2000, v15\nM 76,220,132\nY 98,105,90\nY 111,110,107\nM 76,208,148\n"
"Y 112,110,98\nA 114,33\nM 80,200,148\nA 115,37\nY 116,114,83\nM 80,192,132\ns_waitcnt lgkmcnt(4)\nK 180,-3072\nY 117,91,115\nM 80,196,140\nA 83,41\nA 91,45\nM 80,204,144\n"
"X 118,99,116\nX 119,117,116\nM 84,216,144\nD 144,0,20256\nY 120,114,83\nM 84,212,132\nK 188,-2048\nY 99,115,91\nM 84,220,148\nY 121,120,117\nY 122,120,99\nM 84,208,140\n"
"C 65,108,118\nC 69,109,119\nM 88,200,140\nC 73,111,121\nC 125,112,122\nM 88,192,148\ns_waitcnt lgkmcnt(4)\nL 104,34\nL 105,38\nM 88,196,152\nY 106,104,84\nY 107,92,105\n"
"M 88,204,132\nL 84,42\nL 92,46\nM 92,216,132\nD 132,0,17728\nX 108,100,106\nM 92,212,148\nX 109,107,106\nY 110,104,84\nM 92,220,140\nY 100,105,92\nY 111,110,107\nM 92,208,152\n"
"Y 112,110,100\nA 114,34\nM 96,200,152\nA 115,38\nY 116,114,85\nM 96,192,140\ns_waitcnt lgkmcnt(4)\nY 117,93,115\nA 85,42\nM 96,196,156\nA 93,46\nX 118,101,116\nM 96,204,148\n"
"X 119,117,116\nY 120,114,85\nM 100,216,148\nD 148,0,17984\nY 101,115,93\nM 100,212,140\nY 121,120,117\nY 122,120,101\nM 100,220,152\nC 66,108,118\nC 70,109,119\nM 100,208,156\n"
"C 74,111,121\nC 126,112,122\nM 104,200,156\nL 104,35\nL 105,39\nM 104,192,152\ns_waitcnt lgkmcnt(4)\nY 106,104,86\nY 107,94,105\nM 104,196,128\nL 86,43\nL 94,47\nM 104,204,140\n"
"X 108,102,106\nX 109,107,106\nM 108,216,140\nD 140,0,18240\nY 110,104,86\nM 108,212,152\nY 102,105,94\nY 111,110,107\nM 108,220,156\nY 112,110,102\nA 114,35\nM 108,208,128\n"
"G 32,11,2432\nM 112,200,128\nA 115,39\nY 116,114,87\nM 112,192,156\ns_waitcnt lgkmcnt(4)\nY 117,95,115\nA 87,43\nM 112,196,136\nA 95,47\nX 118,103,116\nM 112,204,152\nG 36,11,3456\n"
"M 116,216,152\nD 152,0,18496\nX 119,117,116\nM 116,212,156\nY 120,114,87\nY 103,115,95\nM 116,220,128\nY 121,120,117\nY 122,120,103\nM 116,208,136\nG 40,12,384\nM 120,200,136\n"
"C 67,108,118\nC 71,109,119\nM 120,192,128\ns_waitcnt lgkmcnt(4)\nC 75,111,121\nC 127,112,122\nM 120,196,144\ns_waitcnt vmcnt(21)\nL 104,48\nL 105,52\nM 120,204,156\nG 44,12,1408\n"
"M 124,216,156\nD 156,0,18752\nY 106,104,80\nM 124,212,128\nD 128,0,19008\nY 107,88,105\nM 124,220,136\ns_waitcnt vmcnt(21)\nD 136,0,19264\nL 80,56\nM 124,208,144\n"
"s_waitcnt vmcnt(12) lgkmcnt(6)\nD 144,0,19520\nL 88,60\nM 64,236,132\ns_waitcnt lgkmcnt(6)\nK 200,-1024\nX 108,96,106\nM 64,224,148\ns_waitcnt lgkmcnt(5)\nX 109,107,106\n"
"Y 110,104,80\nM 64,232,140\ns_waitcnt lgkmcnt(4)\nY 96,105,88\nY 111,110,107\nM 64,228,152\nY 112,110,96\nA 114,48\nM 68,240,152\nK 192,3072\nA 115,52\nM 68,248,132\nD 132,0,19808\n"
"Y 116,114,81\nM 68,244,148\nY 117,89,115\nA 81,56\nM 68,252,140\nA 89,60\nX 118,97,116\nM 72,224,140\nK 196,1024\nX 119,117,116\nM 72,232,152\ns_waitcnt lgkmcnt(4)\nY 120,114,81\n"
"Y 97,115,89\nM 72,228,156\nY 121,120,117\nY 122,120,97\nM 72,236,148\nM 76,248,148\nD 148,0,20064\nM 76,244,140\nK 204,2048\nv_add_u32 v15, 0xa000, v15\nM 76,252,152\nM 76,240,156\n"
"M 80,232,156\nM 80,224,152\ns_waitcnt lgkmcnt(4)\nK 216,-4096\nM 80,228,128\nM 80,236,140\nM 84,248,140\nD 140,0,20320\nM 84,244,152\nK 212,-3072\nM 84,252,156\nM 84,240,128\n"
"M 88,232,128\nM 88,224,156\ns_waitcnt lgkmcnt(0)\ns_barrier\nM 88,228,136\nK 220,-2048\nM 88,236,152\nM 92,248,152\nD 152,1,35584\nM 92,244,156\nS 3,64,0\nM 92,252,128\nK 208,-1024\n"
"C 64,108,118\nM 92,240,136\nM 96,232,136\nS 3,68,17664\nM 96,224,128\nC 68,109,119\nM 96,228,144\nM 96,236,156\nM 100,248,156\nD 156,1,35840\nM 100,244,128\nS 5,72,35328\n"
"M 100,252,136\nC 72,111,121\nC 56,112,122\nM 100,240,144\nL 104,49\nL 105,53\nM 104,232,144\nY 106,104,82\nY 107,90,105\nM 104,224,136\nL 82,57\nL 90,61\nM 104,228,132\n"
"X 108,98,106\nX 109,107,106\nM 104,236,128\nY 110,104,82\nY 98,105,90\nM 108,248,128\nD 128,1,36096\nY 111,110,107\nM 108,244,136\nY 112,110,98\nA 114,49\nM 108,252,144\nA 115,53\n"
"Y 116,114,83\nM 108,240,132\nY 117,91,115\nA 83,57\nM 112,232,132\nA 91,61\nX 118,99,116\nM 112,224,144\nX 119,117,116\nY 120,114,83\nM 112,228,148\nY 99,115,91\nY 121,120,117\n"
"M 112,236,136\nY 122,120,99\nC 65,108,118\nM 116,248,136\nD 136,1,36352\nC 69,109,119\nM 116,244,144\nC 73,111,121\nC 57,112,122\nM 116,252,132\nL 104,50\nL 105,54\nM 116,240,148\n"
"Y 106,104,84\nY 107,92,105\nM 120,232,148\nK 232,3072\nL 84,58\nM 120,224,132\nL 92,62\nX 108,100,106\nM 120,228,140\nX 109,107,106\nY 110,104,84\nM 120,236,144\nY 100,105,92\n"
"Y 111,110,107\nM 124,248,144\nD 144,1,36608\nY 112,110,100\nM 124,244,132\nD 132,1,36864\nA 114,50\nM 124,252,148\nD 148,1,37120\nA 115,54\nM 124,240,140\n.Lpad14:\n"
".if (.Lpad14 - .Lbase) != 57360\n.error \"pad\"\n.endif\ns_cbranch_execz .Lret1\ns_waitcnt lgkmcnt(9)\nD 140,1,37408\nY 116,114,85\nM 0,172,152\ns_waitcnt lgkmcnt(7)\nK 224,0\n"
"Y 117,93,115\nM 0,160,156\ns_waitcnt vmcnt(19) lgkmcnt(5)\nA 85,58\nA 93,62\nM 0,168,128\ns_waitcnt vmcnt(18)\nX 118,101,116\nX 119,117,116\nM 4,164,128\ns_waitcnt vmcnt(17)\n"
"Y 120,114,85\nY 101,115,93\nM 4,176,152\ns_waitcnt vmcnt(16)\nD 152,1,37664\nY 121,120,117\nM 4,184,156\nK 228,1024\nY 122,120,101\nM 8,172,156\nC 66,108,118\nC 70,109,119\n"
"M 8,160,128\ns_waitcnt lgkmcnt(5)\nC 74,111,121\nC 58,112,122\nM 8,168,136\nL 104,51\nL 105,55\nM 12,164,136\nK 236,2048\nv_add_u32 v15, 0x2000, v15\nM 12,176,156\nD 156,1,37920\n"
"Y 106,104,86\nM 12,184,128\nY 107,94,105\nL 86,59\nM 16,172,128\nL 94,63\nX 108,102,106\nM 16,160,136\ns_waitcnt lgkmcnt(5)\nK 248,-4096\nX 109,107,106\nM 16,168,144\nY 110,104,86\n"
"Y 102,105,94\nM 20,164,144\nY 111,110,107\nY 112,110,102\nM 20,176,128\nD 128,1,35648\nA 114,51\nM 20,184,136\nG 48,12,2432\nM 24,172,136\nA 115,55\nY 116,114,87\nM 24,160,144\n"
"s_waitcnt lgkmcnt(5)\nY 117,95,115\nA 87,59\nM 24,168,132\nA 95,63\nX 118,103,116\nM 28,164,132\nG 52,12,3456\nM 28,176,136\nD 136,1,35904\nX 119,117,116\nM 28,184,144\n"
"Y 120,114,87\nY 103,115,95\nM 32,172,144\nY 121,120,117\nY 122,120,103\nM 32,160,132\ns_waitcnt lgkmcnt(5)\nH 17,13,384\nM 32,168,148\nC 67,108,118\nC 71,109,119\nM 36,164,148\n"
"H 18,13,1408\nM 36,176,144\nD 144,1,36160\nC 75,111,121\nM 36,184,132\nH 19,13,2432\nM 40,172,132\nC 59,112,122\nL 104,24\nM 40,160,148\ns_waitcnt lgkmcnt(5)\nH 20,13,3456\n"
"M 40,168,140\nS 3,64,256\nM 44,164,140\nH 21,14,384\nM 44,176,132\nL 105,28\nY 106,104,80\nM 44,184,148\nD 132,1,36416\nY 107,88,105\nM 48,172,148\nH 22,14,1408\nM 48,160,140\n"
"s_waitcnt vmcnt(23) lgkmcnt(6)\nL 80,32\nL 88,36\nM 48,168,152\nH 23,14,2432\nM 52,164,152\nX 108,96,106\nX 109,107,106\nM 52,176,148\nD 148,1,36672\nY 110,104,80\nM 52,184,140\n"
"K 244,-3072\nY 96,105,88\nM 56,172,140\nS 3,68,17920\nM 56,160,152\ns_waitcnt lgkmcnt(7)\nY 111,110,107\nY 112,110,96\nM 56,168,156\nA 114,24\nA 115,28\nM 60,164,156\nD 156,1,36928\n"
"Y 116,114,81\nM 60,176,140\nD 140,1,37184\nY 117,89,115\nM 60,184,152\ns_waitcnt lgkmcnt(8)\nD 152,1,37472\nA 81,32\nM 0,180,128\ns_waitcnt lgkmcnt(8)\nK 252,-2048\nA 89,36\n"
"M 0,188,136\ns_waitcnt vmcnt(23) lgkmcnt(7)\nS 5,72,35584\nM 0,200,144\ns_waitcnt vmcnt(22)\nX 118,97,116\nX 119,117,116\nM 4,192,144\ns_waitcnt vmcnt(21)\nY 120,114,81\n"
"Y 97,115,89\nM 4,196,128\ns_waitcnt vmcnt(20)\nD 128,1,37728\nY 121,120,117\nM 4,204,136\nK 240,-1024\nY 122,120,97\nM 8,180,136\nC 64,108,118\nC 68,109,119\nM 8,188,144\n"
"s_waitcnt lgkmcnt(7)\nC 72,111,121\nC 76,112,122\nM 8,200,132\nL 104,25\nL 105,29\nM 12,192,132\nK 172,3072\nY 106,104,82\nM 12,196,136\nD 136,1,37984\nY 107,90,105\nM 12,204,144\n"
"L 82,33\nL 90,37\nM 16,180,144\nX 108,98,106\nX 109,107,106\nM 16,188,132\ns_waitcnt lgkmcnt(7)\nK 160,0\nY 110,104,82\nM 16,200,148\nY 98,105,90\nY 111,110,107\nM 20,192,148\n"
"s_waitcnt lgkmcnt(0)\ns_barrier\nY 112,110,98\nM 20,196,144\nD 144,0,35328\nA 114,25\nM 20,204,132\nK 168,1024\nA 115,29\nM 24,180,132\nY 116,114,83\nY 117,91,115\nM 24,188,148\n"
"A 83,33\nA 91,37\nM 24,200,156\nX 118,99,116\nX 119,117,116\nM 28,192,156\nK 164,2048\nv_add_u32 v15, 0xffff6000, v15\nM 28,196,132\nD 132,0,35584\nY 120,114,83\nM 28,204,148\n"
"Y 99,115,91\nY 121,120,117\nM 32,180,148\nY 122,120,99\nC 65,108,118\nM 32,188,156\nK 176,-3072\nC 69,109,119\nM 32,200,140\nC 73,111,121\nC 77,112,122\nM 36,192,140\nL 104,26\n"
"L 105,30\nM 36,196,148\nD 148,0,35840\nY 106,104,84\nM 36,204,156\nK 184,-2048\nY 107,92,105\nM 40,180,156\nL 84,34\nL 92,38\nM 40,188,140\nX 108,100,106\nX 109,107,106\n"
"M 40,200,152\nY 110,104,84\nY 100,105,92\nM 44,192,152\nY 111,110,107\nY 112,110,100\nM 44,196,156\nD 156,0,36096\nA 114,26\nM 44,204,140\nA 115,30\nY 116,114,85\nM 48,180,140\n"
"Y 117,93,115\nA 85,34\nM 48,188,152\nA 93,38\nX 118,101,116\nM 48,200,128\nX 119,117,116\nY 120,114,85\nM 52,192,128\nY 101,115,93\nY 121,120,117\nM 52,196,140\nD 140,0,36352\n"
"Y 122,120,101\nM 52,204,152\nC 66,108,118\nC 70,109,119\nM 56,180,152\nK 180,-1024\nv_add_u32 v15, 0x2000, v15\nM 56,188,128\nC 74,111,121\nC 78,112,122\nM 56,200,136\nL 104,27\n"
"L 105,31\nM 60,192,136\nD 136,0,36608\nY 106,104,86\nM 60,196,152\nD 152,0,36864\nY 107,94,105\nM 60,204,128\ns_waitcnt vmcnt(27) lgkmcnt(6)\nD 128,0,37120\nL 86,35\nN 128,216,144\n"
"s_waitcnt vmcnt(26) lgkmcnt(6)\nK 188,-3072\nL 94,39\nN 128,212,132\ns_waitcnt vmcnt(26) lgkmcnt(5)\nX 108,102,106\nX 109,107,106\nN 128,220,148\ns_waitcnt vmcnt(25) lgkmcnt(4)\n"
"Y 110,104,86\nY 102,105,94\nN 128,208,156\ns_waitcnt vmcnt(24)\nY 111,110,107\nY 112,110,102\nN 132,232,156\ns_waitcnt vmcnt(23)\nK 200,-2048\nA 114,27\nN 132,224,144\n"
"s_waitcnt vmcnt(23)\nD 144,0,37408\nA 115,31\nN 132,228,132\ns_waitcnt vmcnt(22)\nY 116,114,87\nY 117,95,115\nN 132,236,148\nA 87,35\nA 95,39\nN 136,212,148\nK 192,-1024\n"
"v_add_u32 v15, 0xffffe000, v15\nN 136,220,156\ns_waitcnt lgkmcnt(4)\nX 118,103,116\nX 119,117,116\nN 136,208,140\nY 120,114,87\nY 103,115,95\nN 136,216,132\nY 121,120,117\n"
"Y 122,120,103\nN 140,224,132\nD 132,0,37664\nC 67,108,118\nN 140,228,148\nK 196,1024\nv_add_u32 v15, 0x2000, v15\nN 140,236,156\nC 71,109,119\nC 75,111,121\nN 140,232,140\n"
"C 79,112,122\nL 104,40\nN 144,220,140\nS 3,64,512\nN 144,212,156\ns_waitcnt lgkmcnt(5)\nK 204,1024\nv_add_u32 v15, 0xffffe000, v15\nN 144,208,136\nL 105,44\nY 106,104,80\n"
"N 144,216,148\ns_waitcnt vmcnt(23)\nY 107,88,105\nL 80,48\nN 148,224,148\ns_waitcnt vmcnt(22)\nD 148,0,37920\nL 88,52\nN 148,228,156\nS 3,68,18176\nN 148,236,140\nX 108,96,106\n"
"X 109,107,106\nN 148,232,136\nY 110,104,80\nY 96,105,88\nN 152,220,136\nS 5,72,35840\nN 152,212,140\ns_waitcnt lgkmcnt(7)\nY 111,110,107\nY 112,110,96\nN 152,208,152\nA 114,40\n"
"A 115,44\nN 152,216,156\nY 116,114,81\nY 117,89,115\nN 156,224,156\nD 156,0,35392\nA 81,48\nN 156,228,140\nS 4,76,35840\nN 156,236,136\nA 89,52\nX 118,97,116\nN 156,232,152\n"
"X 119,117,116\nY 120,114,81\nN 160,220,152\nS 4,124,35328\nN 160,212,136\ns_waitcnt lgkmcnt(9)\nY 97,115,89\nY 121,120,117\nN 160,208,128\nY 122,120,97\nC 64,108,118\nN 160,216,140\n"
"C 68,109,119\nC 72,111,121\nN 164,224,140\nD 140,0,35648\nC 76,112,122\nN 164,228,136\nS 4,56,35584\nN 164,236,152\nL 104,41\nL 105,45\nN 164,232,128\nY 106,104,82\nY 107,90,105\n"
"N 168,220,128\nL 82,49\nL 90,53\nN 168,212,152\ns_waitcnt lgkmcnt(10)\nX 108,98,106\nX 109,107,106\nN 168,208,144\nY 110,104,82\nY 98,105,90\nN 168,216,136\nY 111,110,107\n"
"Y 112,110,98\nN 172,224,136\nD 136,0,35904\nA 114,41\nN 172,228,152\nA 115,45\nY 116,114,83\nN 172,236,128\nY 117,91,115\nA 83,49\nN 172,232,144\nA 91,53\nX 118,99,116\n"
"N 176,220,144\nX 119,117,116\nY 120,114,83\nN 176,212,128\ns_waitcnt lgkmcnt(10)\nY 99,115,91\nY 121,120,117\nN 176,208,132\nY 122,120,99\nC 65,108,118\nN 176,216,152\nC 69,109,119\n"
"C 73,111,121\nN 180,224,152\nD 152,0,36160\nC 77,112,122\nN 180,228,128\nL 104,42\nL 105,46\nN 180,236,144\nY 106,104,84\nY 107,92,105\nN 180,232,132\nL 84,50\nL 92,54\n"
"N 184,220,132\nK 220,2048\nv_add_u32 v15, 0x2000, v15\nN 184,212,144\ns_waitcnt lgkmcnt(9)\nX 108,100,106\nX 109,107,106\nN 184,208,148\nY 110,104,84\nY 100,105,92\nN 184,216,128\n"
"Y 111,110,107\nY 112,110,100\nN 188,224,128\nD 128,0,36416\nA 114,42\nN 188,228,144\nD 144,0,36672\nA 115,46\nN 188,236,132\nD 132,0,36928\nY 116,114,85\nN 188,232,148\n"
"s_waitcnt lgkmcnt(9)\nD 148,0,37184\nY 117,93,115\nN 128,248,156\ns_waitcnt vmcnt(15) lgkmcnt(7)\nK 212,2048\nv_add_u32 v15, 0xffffe000, v15\nN 128,244,140\n"
"s_waitcnt vmcnt(15) lgkmcnt(5)\nA 85,50\nA 93,54\nN 128,252,136\ns_waitcnt vmcnt(14) lgkmcnt(4)\nX 118,101,116\nX 119,117,116\nN 128,240,152\ns_waitcnt vmcnt(13)\nY 120,114,85\n"
"Y 101,115,93\nN 132,172,152\ns_waitcnt vmcnt(12)\nK 208,3072\nv_add_u32 v15, 0x2000, v15\nN 132,160,156\ns_waitcnt vmcnt(12)\nD 156,0,37472\nY 121,120,117\nN 132,168,140\n"
"s_waitcnt vmcnt(11)\nY 122,120,101\nC 66,108,118\nN 132,164,136\nC 70,109,119\nC 74,111,121\nN 136,244,136\nK 216,3072\nv_add_u32 v15, 0x2a000, v15\nN 136,252,152\n"
"s_waitcnt lgkmcnt(4)\nC 78,112,122\nL 104,43\nN 136,240,128\nL 105,47\nY 106,104,86\nN 136,248,140\nY 107,94,105\nL 86,51\nN 140,160,140\nD 140,0,37728\nL 94,55\nN 140,168,136\n"
"K 224,-4096\nX 108,102,106\nN 140,164,152\nX 109,107,106\nY 110,104,86\nN 140,172,128\nY 102,105,94\nY 111,110,107\nN 144,252,128\nY 112,110,102\nA 114,43\nN 144,244,152\n"
"s_waitcnt lgkmcnt(4)\nK 228,-3072\nA 115,47\nN 144,240,144\nY 116,114,87\nY 117,95,115\nN 144,248,136\nA 87,51\nA 95,55\nN 148,160,136\nD 136,0,37984\nX 118,103,116\nN 148,168,152\n"
"K 236,-2048\nX 119,117,116\nN 148,164,128\nY 120,114,87\nY 103,115,95\nN 148,172,144\nY 121,120,117\nY 122,120,103\nN 152,252,144\nC 67,108,118\nC 71,109,119\nN 152,244,128\n"
"s_waitcnt lgkmcnt(4)\nK 232,-1024\nC 75,111,121\nN 152,240,132\nC 79,112,122\nL 80,17\nN 152,248,152\nS 3,64,768\nN 156,160,152\nL 81,18\nL 82,19\nN 156,168,128\nY 80,80,82\n"
"L 104,20\nN 156,164,144\nL 105,21\nY 106,104,81\nN 156,172,132\nY 107,82,105\nL 81,22\nN 160,252,132\nS 3,68,18432\nN 160,244,144\ns_waitcnt lgkmcnt(5)\nL 82,23\nX 108,80,106\n"
"N 160,240,148\nX 109,107,106\nY 110,104,81\nN 160,248,128\nY 80,105,82\nY 111,110,107\nN 164,160,128\nY 112,110,80\nA 84,17\nN 164,168,144\nS 5,72,36096\nN 164,164,132\nA 85,18\n"
"A 86,19\nN 164,172,148\nY 84,84,86\nA 114,20\nN 168,252,148\nA 115,21\nY 116,114,85\nN 168,244,132\ns_waitcnt lgkmcnt(5)\nY 117,86,115\nA 85,22\nN 168,240,156\nS 4,76,36096\n"
"N 168,248,144\nA 86,23\nX 118,84,116\nN 172,160,144\nX 119,117,116\nY 120,114,85\nN 172,168,132\nY 84,115,86\nY 121,120,117\nN 172,164,148\nY 122,120,84\nC 64,108,118\n"
"N 172,172,156\nC 65,109,119\nC 66,111,121\nN 176,252,156\nC 67,112,122\nN 176,244,148\ns_waitcnt lgkmcnt(5)\nU 6,64,0\nN 176,240,140\nU 6,65,17664\nN 176,248,132\nU 123,66,0\n"
"N 180,160,132\n.Lpad15:\n.if (.Lpad15 - .Lbase) != 61444\n.error \"pad\"\n.endif\ns_cbranch_execz .Lret1\nU 7,67,17664\nN 180,168,148\nv_mbcnt_lo_u32_b32 v10, -1, 0\n"
"v_mbcnt_hi_u32_b32 v10, -1, v10\nN 180,164,156\nv_lshrrev_b32 v11, 5, v10\nv_lshl_add_u32 v11, s16, 1, v11\nN 180,172,140\ns_waitcnt lgkmcnt(0)\ns_barrier\nv_xor_b32 v11, v11, v10\n"
"N 184,252,140\nD 152,0,256\nv_and_b32 v11, 7, v11\nN 184,244,156\nD 128,0,512\nv_and_b32 v12, 15, v10\nN 184,240,136\ns_mov_b64 exec, 0\ns_cmp_eq_u32 s16, 1\ns_cbranch_scc1 .Lpad17\n"
"s_cmp_eq_u32 s16, 2\ns_cbranch_scc1 .Lpad18\ns_cmp_eq_u32 s16, 3\ns_cbranch_scc1 .Lpad19\ns_branch .Lpad16\n.Lret2:\ns_mov_b64 exec, -1\nD 144,0,768\n"
"v_lshl_add_u32 v11, v12, 3, v11\nN 184,248,148\nD 132,0,320\nv_bfe_u32 v12, v10, 4, 1\nN 188,160,148\nD 148,0,576\nv_lshl_add_u32 v11, v11, 1, v12\nN 188,168,156\nD 156,0,832\n"
"v_lshl_add_u32 v8, v11, 3, s57\nN 188,164,140\nD 140,0,1024\nv_lshrrev_b32 v11, 3, v10\nN 188,172,136\ns_waitcnt vmcnt(15) lgkmcnt(6)\nD 136,0,1088\nv_xor_b32 v11, v11, v10\n"
"M 0,176,152\ns_waitcnt vmcnt(14) lgkmcnt(6)\nK 252,3072\nv_lshl_add_u32 v9, v11, 4, s58\nM 0,184,128\ns_waitcnt vmcnt(14) lgkmcnt(5)\nM 0,180,144\ns_waitcnt vmcnt(13) lgkmcnt(4)\n"
"M 0,188,132\ns_waitcnt vmcnt(12) lgkmcnt(3)\nM 0,200,148\ns_waitcnt vmcnt(11) lgkmcnt(2)\nK 244,0\nM 0,192,156\ns_waitcnt vmcnt(11)\nM 4,196,152\ns_waitcnt vmcnt(10)\nD 152,0,1280\n"
"M 4,204,132\ns_waitcnt vmcnt(9)\nD 132,0,1344\nM 4,220,128\ns_waitcnt vmcnt(8)\nK 240,1024\nM 4,212,148\ns_waitcnt vmcnt(8)\nM 4,208,144\ns_waitcnt vmcnt(7)\nM 4,216,156\n"
"M 8,200,156\nK 248,2048\nv_add_u32 v15, 0x2000, v15\nM 8,176,128\nM 8,188,148\nM 8,184,144\ns_waitcnt lgkmcnt(3)\nRA 24,0\nRA 28,4\nM 8,180,140\ns_waitcnt lgkmcnt(2)\nK 160,-4096\n"
"RA 25,1\nM 8,192,136\nRA 29,5\nRA 26,2\nM 12,196,128\nD 128,0,1536\nRA 30,6\nM 12,204,148\nD 148,0,1600\nRA 27,3\nM 12,220,144\nK 168,-3072\nRA 31,7\nM 12,212,156\nEA 36,24\n"
"M 12,208,140\nEA 37,25\nM 12,216,136\nEA 38,26\nM 16,200,136\nK 164,-2048\nE3 40,36\nM 16,176,144\nEA 39,27\nM 16,188,156\nEX 28,28\nM 16,184,140\ns_waitcnt lgkmcnt(3)\nEX 29,29\n"
"M 16,180,152\ns_waitcnt lgkmcnt(2)\nK 172,-1024\nE3 41,37\nM 16,192,132\nEX 30,30\nM 20,196,144\nD 144,0,1792\nE3 42,38\nM 20,204,156\nD 156,0,1856\nE3 43,39\nM 20,220,140\n"
"EX 31,31\nM 20,212,136\nE4 28,28,40\nE4 29,29,41\nM 20,208,152\nE4 30,30,42\nE4 31,31,43\nM 20,216,132\nE5 36,40\nE5 37,41\nM 24,200,132\nE5 38,42\nE5 39,43\nM 24,176,140\nRC 28,28\n"
"M 24,188,136\nRC 29,29\nM 24,184,152\ns_waitcnt lgkmcnt(3)\nRC 30,30\nM 24,180,128\ns_waitcnt lgkmcnt(2)\nRC 31,31\nM 24,192,148\nE6 36,24,36\nE6 37,25,37\nM 28,196,140\n"
"D 140,0,2080\nE6 38,26,38\nM 28,204,136\nD 136,0,2144\nE6 39,27,39\nM 28,220,152\nZ 36,36,28\nZ 37,37,29\nM 28,212,132\nZ 38,38,30\nZ 39,39,31\nM 28,208,128\nC 72,36,37\nC 73,38,39\n"
"M 28,216,148\nT 72,0\nM 32,200,148\nRA 48,8\nRA 52,12\nM 32,176,152\nRA 49,9\nRA 53,13\nM 32,188,132\nRA 50,10\nRA 54,14\nM 32,184,128\ns_waitcnt lgkmcnt(4)\nRA 51,11\nRA 55,15\n"
"M 32,180,144\ns_waitcnt lgkmcnt(3)\nEA 60,48\nM 32,192,156\nEA 61,49\nM 36,196,152\nD 152,0,2336\nE3 64,60\nM 36,204,132\nD 132,0,2400\nE3 65,61\nM 36,220,128\nEA 62,50\n"
"M 36,212,148\nEA 63,51\nM 36,208,144\nEX 52,52\nM 36,216,156\nEX 53,53\nM 40,200,156\nEX 54,54\nM 40,176,128\nEX 55,55\nM 40,188,148\nE3 66,62\nE3 67,63\nM 40,184,144\n"
"s_waitcnt lgkmcnt(4)\nE4 52,52,64\nE4 53,53,65\nM 40,180,140\ns_waitcnt lgkmcnt(3)\nE4 54,54,66\nE4 55,55,67\nM 40,192,136\nE5 60,64\nE5 61,65\nM 44,196,128\nD 128,0,2592\nE5 62,66\n"
"M 44,204,148\nD 148,0,2656\nE5 63,67\nM 44,220,144\nRC 52,52\nM 44,212,156\nRC 53,53\nM 44,208,140\nRC 54,54\nM 44,216,136\nRC 55,55\nM 48,200,136\nE6 60,48,60\nE6 61,49,61\n"
"M 48,176,144\nE6 62,50,62\nE6 63,51,63\nM 48,188,156\nZ 60,60,52\nZ 61,61,53\nM 48,184,140\ns_waitcnt lgkmcnt(3)\nZ 62,62,54\nZ 63,63,55\nM 48,180,152\ns_waitcnt lgkmcnt(2)\n"
"C 74,60,61\nC 75,62,63\nM 48,192,132\nT 74,2048\nM 52,196,144\nD 144,1,0\nRA 24,16\nM 52,204,156\nD 156,1,256\nRA 28,20\nM 52,220,140\nRA 25,17\nRA 29,21\nM 52,212,136\nRA 26,18\n"
"RA 30,22\nM 52,208,152\nRA 27,19\nRA 31,23\nM 52,216,132\nEA 36,24\nM 56,200,132\nK 200,3072\nE3 40,36\nM 56,176,140\nEA 37,25\nM 56,188,136\nEA 38,26\nM 56,184,152\n"
"s_waitcnt lgkmcnt(4)\nEA 39,27\nM 56,180,128\ns_waitcnt lgkmcnt(3)\nK 176,0\nE3 41,37\nM 56,192,148\nEX 28,28\nM 60,196,140\nD 140,1,512\nE3 42,38\nM 60,204,136\nD 136,1,768\n"
"E3 43,39\nM 60,220,152\nD 152,1,1024\nE4 28,28,40\nM 60,212,132\nD 132,1,1280\nE5 36,40\nM 60,208,128\nD 128,1,1536\nE5 37,41\nM 60,216,148\ns_waitcnt vmcnt(13) lgkmcnt(6)\n"
"D 148,1,1792\nE5 38,42\nM 64,224,144\ns_waitcnt vmcnt(12) lgkmcnt(6)\nK 188,1024\nE5 39,43\nM 64,228,156\ns_waitcnt vmcnt(12) lgkmcnt(5)\nEX 29,29\nM 64,236,140\n"
"s_waitcnt vmcnt(11) lgkmcnt(4)\nEX 30,30\nM 64,232,136\ns_waitcnt vmcnt(10)\nEX 31,31\nM 68,252,136\ns_waitcnt vmcnt(9)\nK 184,2048\nv_add_u32 v15, 0x2000, v15\nM 68,244,144\n"
"s_waitcnt vmcnt(9)\nD 144,1,2080\nE4 29,29,41\nM 68,240,156\ns_waitcnt vmcnt(8)\nE4 30,30,42\nE4 31,31,43\nM 68,248,140\nRC 28,28\nM 72,228,140\nK 180,-4096\nE6 36,24,36\n"
"M 72,236,136\ns_waitcnt lgkmcnt(4)\nRC 29,29\nM 72,232,152\nRC 30,30\nM 72,224,156\nRC 31,31\nM 76,244,156\nD 156,1,2336\nE6 37,25,37\nM 76,240,140\nK 192,-3072\nE6 38,26,38\n"
"M 76,248,136\nE6 39,27,39\nZ 36,36,28\nM 76,252,152\nZ 37,37,29\nZ 38,38,30\nM 80,236,152\nZ 39,39,31\nC 76,36,37\nM 80,228,136\ns_waitcnt lgkmcnt(4)\nK 196,-2048\nC 77,38,39\n"
"M 80,232,132\nT 76,4096\nM 80,224,140\nRA 48,24\nRA 52,28\nM 84,244,140\nD 140,1,2592\nRA 49,25\nM 84,240,136\nK 204,-1024\nv_add_u32 v15, 0x2000, v15\nM 84,248,152\nRA 53,29\n"
"RA 50,26\nM 84,252,132\nRA 54,30\nRA 51,27\nM 88,236,132\nRA 55,31\nRA 24,32\nM 88,228,152\ns_waitcnt lgkmcnt(5)\nK 220,-4096\nRA 28,36\nM 88,232,128\nEA 60,48\nM 88,224,136\n"
"EA 61,49\nM 92,244,136\nD 136,1,64\nE3 64,60\nM 92,240,152\nK 212,-3072\nE3 65,61\nM 92,248,132\nEA 62,50\nM 92,252,128\nEA 63,51\nM 96,236,128\nEX 52,52\nM 96,228,132\n"
"s_waitcnt lgkmcnt(5)\nK 208,-2048\nE3 66,62\nM 96,232,148\nEX 53,53\nM 96,224,152\nEX 54,54\nM 100,244,152\nD 152,1,320\nE3 67,63\nM 100,240,132\nK 216,-1024\n"
"v_add_u32 v15, 0xffffe000, v15\nM 100,248,128\nEX 55,55\nM 100,252,148\nE4 52,52,64\nE4 53,53,65\nM 104,236,148\nE4 54,54,66\nE4 55,55,67\nM 104,228,128\ns_waitcnt lgkmcnt(5)\n"
"E5 60,64\nE5 61,65\nM 104,232,144\nE5 62,66\nE5 63,67\nM 104,224,132\nRC 52,52\nM 108,244,132\nD 132,1,576\nE6 60,48,60\nM 108,240,128\nRC 53,53\nM 108,248,148\nRC 54,54\n"
"M 108,252,144\nRC 55,55\nM 112,236,144\nE6 61,49,61\nE6 62,50,62\nM 112,228,148\ns_waitcnt lgkmcnt(5)\nE6 63,51,63\nZ 60,60,52\nM 112,232,156\nZ 61,61,53\nZ 62,62,54\nM 112,224,128\n"
"Z 63,63,55\nC 78,60,61\nM 116,244,128\nD 128,1,832\nC 79,62,63\nM 116,240,148\nT 78,6144\nM 116,248,144\nRA 25,33\nRA 29,37\nM 116,252,156\nRA 26,34\nRA 30,38\nM 120,236,156\n"
"K 236,0\nv_add_u32 v15, 0x2000, v15\nM 120,228,144\ns_waitcnt lgkmcnt(5)\nRA 27,35\nRA 31,39\nM 120,232,140\nEA 36,24\nM 120,224,148\nEA 37,25\nM 124,244,148\nD 148,1,1088\n"
"E3 40,36\nM 124,240,144\nD 144,1,1344\nE3 41,37\nM 124,248,156\nD 156,1,1600\nE5 36,40\nM 124,252,140\ns_waitcnt vmcnt(16) lgkmcnt(7)\nD 140,1,1856\nE5 37,41\nM 64,160,136\n"
"s_waitcnt vmcnt(15) lgkmcnt(7)\nK 228,0\nv_add_u32 v15, 0xffffe000, v15\nM 64,168,152\ns_waitcnt vmcnt(15) lgkmcnt(6)\nEA 38,26\nM 64,164,132\ns_waitcnt vmcnt(14) lgkmcnt(5)\n"
"EA 39,27\nM 64,172,128\ns_waitcnt vmcnt(13)\nEX 28,28\nM 68,200,128\ns_waitcnt vmcnt(12)\nK 232,1024\nv_add_u32 v15, 0x2000, v15\nM 68,176,136\ns_waitcnt vmcnt(12)\nD 136,1,2144\n"
"E3 42,38\nM 68,188,152\ns_waitcnt vmcnt(11)\nEX 29,29\nM 68,184,132\nEX 30,30\nM 72,168,132\nK 224,1024\nv_add_u32 v15, 0xffffe000, v15\nM 72,164,128\ns_waitcnt lgkmcnt(4)\n"
"EX 31,31\nM 72,172,148\nE3 43,39\nE4 28,28,40\nM 72,160,152\nE4 29,29,41\nE4 30,30,42\nM 76,176,152\nD 152,1,2400\nE4 31,31,43\nM 76,188,132\nK 244,2048\nv_add_u32 v15, 0x2000, v15\n"
"M 76,184,128\nE5 38,42\nE5 39,43\nM 76,200,148\nRC 28,28\nM 80,164,148\nRC 29,29\nM 80,168,128\ns_waitcnt lgkmcnt(4)\nK 240,2048\nv_add_u32 v15, 0xffffe000, v15\nM 80,172,144\n"
"RC 30,30\nM 80,160,132\nRC 31,31\nM 84,176,132\nD 132,1,2656\nE6 36,24,36\nM 84,188,128\nK 248,3072\nv_add_u32 v15, 0x2000, v15\nM 84,184,148\nE6 37,25,37\nE6 38,26,38\n"
"M 84,200,144\nE6 39,27,39\nZ 36,36,28\nM 88,164,144\nZ 37,37,29\nZ 38,38,30\nM 88,168,148\ns_waitcnt lgkmcnt(4)\nK 252,3072\nv_add_u32 v15, 0xfffda000, v15\nM 88,172,156\n"
"Z 39,39,31\nC 72,36,37\nM 88,160,128\nC 73,38,39\nRA 48,40\nM 92,176,128\nD 128,1,35328\nRA 52,44\nM 92,188,148\nT 72,8192\nM 92,184,144\nRA 49,41\nRA 53,45\nM 92,200,156\nRA 50,42\n"
"RA 54,46\nM 96,164,156\n.Lpad16:\n.if (.Lpad16 - .Lbase) != 65540\n.error \"pad\"\n.endif\ns_cbranch_execz .Lret2\nRA 51,43\nRA 55,47\nM 96,168,144\ns_waitcnt lgkmcnt(5)\nEA 60,48\n"
"M 96,172,140\nEA 61,49\nM 96,160,148\nEA 62,50\nM 100,176,148\nD 148,1,35584\nE3 64,60\nM 100,188,144\nEA 63,51\nM 100,184,156\nEX 52,52\nM 100,200,140\nEX 53,53\nM 104,164,140\n"
"EX 54,54\nM 104,168,156\ns_waitcnt lgkmcnt(5)\nEX 55,55\nM 104,172,136\nE3 65,61\nE3 66,62\nM 104,160,144\nE3 67,63\nE4 52,52,64\nM 108,176,144\nD 144,1,35840\nE4 53,53,65\n"
"M 108,188,156\nE4 54,54,66\nE4 55,55,67\nM 108,184,140\nE5 60,64\nE5 61,65\nM 108,200,136\nE5 62,66\nE5 63,67\nM 112,164,136\nRC 52,52\nM 112,168,140\ns_waitcnt lgkmcnt(5)\n"
"RC 53,53\nM 112,172,152\nRC 54,54\nM 112,160,156\nRC 55,55\nM 116,176,156\nD 156,1,36096\nE6 60,48,60\nM 116,188,140\nE6 61,49,61\nE6 62,50,62\nM 116,184,136\nE6 63,51,63\n"
"Z 60,60,52\nM 116,200,152\nZ 61,61,53\nZ 62,62,54\nM 120,164,152\nK 164,-4096\nZ 63,63,55\nM 120,168,136\ns_waitcnt lgkmcnt(5)\nC 74,60,61\nC 75,62,63\nM 120,172,132\nT 74,10240\n"
"M 120,160,140\nRA 24,48\nRA 28,52\nM 124,176,140\nD 140,1,35392\nRA 25,49\nM 124,188,136\nD 136,1,35648\nRA 29,53\nM 124,184,152\nD 152,1,35904\nRA 26,50\nM 124,200,132\n"
"s_waitcnt vmcnt(16) lgkmcnt(8)\nD 132,1,36160\nRA 30,54\nM 64,180,128\ns_waitcnt vmcnt(15) lgkmcnt(7)\nK 168,-3072\nRA 27,51\nM 64,192,148\ns_waitcnt vmcnt(15) lgkmcnt(6)\nRA 31,55\n"
"RA 48,56\nM 64,196,144\ns_waitcnt vmcnt(14) lgkmcnt(5)\nEA 36,24\nM 64,204,156\ns_waitcnt vmcnt(13) lgkmcnt(3)\nEA 37,25\nM 64,220,140\ns_waitcnt vmcnt(12) lgkmcnt(2)\nK 172,-2048\n"
"E3 40,36\nM 64,212,136\ns_waitcnt vmcnt(12) lgkmcnt(1)\nEA 38,26\nM 64,208,152\ns_waitcnt vmcnt(11) lgkmcnt(0)\nEA 39,27\nM 64,216,132\ns_waitcnt vmcnt(10)\nEX 28,28\nM 68,236,128\n"
"s_waitcnt vmcnt(9)\nD 128,1,36352\nE3 41,37\nM 68,228,140\ns_waitcnt vmcnt(8)\nD 140,1,36416\nE3 42,38\nM 68,232,148\ns_waitcnt vmcnt(7)\nK 160,-1024\nE3 43,39\nM 68,224,136\n"
"s_waitcnt vmcnt(7)\nEX 29,29\nM 68,244,144\ns_waitcnt vmcnt(6)\nEX 30,30\nM 68,240,152\ns_waitcnt vmcnt(5)\nEX 31,31\nM 68,248,156\ns_waitcnt vmcnt(4)\nK 176,3072\nE4 28,28,40\n"
"M 68,252,132\nE4 29,29,41\nE4 30,30,42\nM 72,208,132\nE4 31,31,43\nE5 36,40\nM 72,180,148\nE5 37,41\nE5 38,42\nM 72,220,136\nK 188,0\nE5 39,43\nM 72,192,144\nRC 28,28\nM 72,212,152\n"
"RC 29,29\nM 72,196,156\ns_waitcnt lgkmcnt(1)\nRC 30,30\nM 72,204,128\ns_waitcnt lgkmcnt(0)\nK 184,1024\nE6 36,24,36\nM 72,216,140\nRC 31,31\nM 76,236,148\nD 148,1,36608\n"
"E6 37,25,37\nM 76,228,136\nD 136,1,36672\nE6 38,26,38\nM 76,232,144\nK 200,2048\nv_add_u32 v15, 0x2000, v15\nM 76,224,152\nE6 39,27,39\nZ 36,36,28\nM 76,244,156\nZ 37,37,29\n"
"Z 38,38,30\nM 76,240,132\nZ 39,39,31\nC 76,36,37\nM 76,248,128\nC 77,38,39\nRA 52,60\nM 76,252,140\nT 76,12288\nM 80,208,140\nRA 49,57\nRA 53,61\nM 80,180,144\nRA 50,58\nRA 54,62\n"
"M 80,220,152\nRA 51,59\nRA 55,63\nM 80,192,156\nEA 60,48\nM 80,212,132\nEA 61,49\nM 80,196,128\ns_waitcnt lgkmcnt(2)\nEA 62,50\nM 80,204,148\ns_waitcnt lgkmcnt(1)\nEA 63,51\n"
"M 80,216,136\nEX 52,52\nM 84,236,144\nD 144,1,36864\nE3 64,60\nM 84,228,152\nD 152,1,36928\nE3 65,61\nM 84,232,156\nEX 53,53\nM 84,224,132\nEX 54,54\nM 84,244,128\nEX 55,55\n"
"M 84,240,140\nE3 66,62\nE3 67,63\nM 84,248,148\nE4 52,52,64\nE4 53,53,65\nM 84,252,136\nE4 54,54,66\nE4 55,55,67\nM 88,208,136\nE5 60,64\nE5 61,65\nM 88,180,156\nE5 62,66\nE5 63,67\n"
"M 88,220,132\nRC 52,52\nM 88,192,128\nRC 53,53\nM 88,212,140\nRC 54,54\nM 88,196,148\ns_waitcnt lgkmcnt(1)\nRC 55,55\nM 88,204,144\ns_waitcnt lgkmcnt(0)\nE6 60,48,60\nE6 61,49,61\n"
"M 88,216,152\nE6 62,50,62\nE6 63,51,63\nM 92,236,156\nD 156,1,37120\nZ 60,60,52\nM 92,228,132\nD 132,1,37184\nZ 61,61,53\nM 92,232,128\nD 0,1,37408\nZ 62,62,54\nM 92,224,140\n"
"D 4,1,37472\nZ 63,63,55\nM 92,244,148\nD 8,1,37664\nC 78,60,61\nM 92,240,136\nD 12,1,37728\nC 79,62,63\nM 92,248,144\nD 16,1,37920\nRA 24,64\nM 92,252,152\nD 20,1,37984\nRA 28,68\n"
"M 96,208,152\nD 24,1,17664\nRA 25,65\nM 96,180,128\nD 28,1,17920\nRA 29,69\nM 96,220,140\nD 32,1,18176\nRA 26,66\nM 96,192,148\nD 36,1,18432\nRA 30,70\nM 96,212,136\nD 40,1,18688\n"
"RA 27,67\nM 96,196,144\ns_waitcnt lgkmcnt(12)\nD 44,1,18944\nRA 31,71\nM 96,204,156\ns_waitcnt lgkmcnt(12)\nK 48,-4096\nRA 48,72\nM 96,216,132\nT 78,14336\nM 100,236,128\n"
"D 128,1,19200\nRA 52,76\nM 100,228,140\nD 140,1,19456\nRA 49,73\nM 100,232,148\nK 52,-3072\nRA 53,77\nM 100,224,136\ns_waitcnt lgkmcnt(2)\ns_barrier\nRA 50,74\nM 100,244,144\n"
"ds_read_b128 v[80:83], v9 offset:0\nRA 54,78\nM 100,240,152\nEA 36,24\nM 100,248,156\nK 56,-2048\nE3 40,36\nM 100,252,132\nEA 37,25\nM 104,208,132\nEA 38,26\nM 104,180,148\n"
"EA 39,27\nM 104,220,136\nK 60,-1024\nE3 41,37\nM 104,192,144\nEX 28,28\nM 104,212,152\nEX 29,29\nM 104,196,156\nEX 30,30\nM 104,204,0\nEX 31,31\nM 104,216,4\nE3 42,38\nE3 43,39\n"
"M 108,236,148\nD 148,1,19744\nE4 28,28,40\nM 108,228,136\nD 136,1,20000\nE4 29,29,41\nM 108,232,144\nE4 30,30,42\nE4 31,31,43\nM 108,224,152\nE5 36,40\nE5 37,41\nM 108,244,156\n"
"E5 38,42\nE5 39,43\nM 108,240,132\nRC 28,28\nM 108,248,0\nRC 29,29\nM 108,252,4\nRC 30,30\nM 112,208,4\nRC 31,31\nM 112,180,144\nE6 36,24,36\nE6 37,25,37\nM 112,220,152\n"
"E6 38,26,38\nE6 39,27,39\nM 112,192,156\nZ 36,36,28\nZ 37,37,29\nM 112,212,132\nZ 38,38,30\nZ 39,39,31\nM 112,196,0\ns_waitcnt lgkmcnt(2)\nP 80,48,0\nM 112,204,8\nC 72,36,37\n"
"C 73,38,39\nM 112,216,12\nT 72,17664\nM 116,236,144\nD 144,1,20256\nRA 51,75\nM 116,228,152\nD 152,1,17728\nRA 55,79\nM 116,232,156\nEA 60,48\nM 116,224,132\nEA 61,49\nM 116,244,0\n"
"EA 62,50\nM 116,240,4\nEA 63,51\nM 116,248,8\nEX 52,52\nM 116,252,12\nEX 53,53\nM 120,208,12\nK 208,3072\nE3 64,60\nM 120,180,156\nEX 54,54\nM 120,220,132\nEX 55,55\nM 120,192,0\n"
"E3 65,61\nE3 66,62\nM 120,212,4\nK 180,0\nE3 67,63\nM 120,196,8\nE4 52,52,64\nE4 53,53,65\nM 120,204,16\nE4 54,54,66\nE4 55,55,67\nM 120,216,20\nE5 60,64\nE5 61,65\nM 124,236,156\n"
"D 156,1,17984\nE5 62,66\nM 124,228,132\nD 132,1,18240\nE5 63,67\nM 124,232,0\nD 0,1,18496\nE6 60,48,60\nM 124,224,4\nD 4,1,18752\nE6 61,49,61\nM 124,244,8\nD 8,1,19008\nE6 62,50,62\n"
"M 124,240,12\nD 12,1,19264\nE6 63,51,63\nM 124,248,16\nD 16,1,19520\nRA 24,80\nM 124,252,20\ns_waitcnt vmcnt(14)\nD 20,1,19808\nRA 28,84\nN 128,164,24\ns_waitcnt vmcnt(13)\n"
"K 220,1024\nRA 25,81\nN 128,168,28\ns_waitcnt vmcnt(13)\nRC 52,52\nN 128,172,32\ns_waitcnt vmcnt(12)\nRC 53,53\nN 128,160,36\ns_waitcnt vmcnt(11)\nRC 54,54\nN 132,176,36\n"
"s_waitcnt vmcnt(10)\nK 192,2048\nv_add_u32 v15, 0x2000, v15\nN 132,188,24\ns_waitcnt vmcnt(10)\nD 24,1,20064\nZ 60,60,52\nN 132,184,28\ns_waitcnt vmcnt(9)\nRC 55,55\nN 132,200,32\n"
"Z 61,61,53\nZ 62,62,54\nN 136,168,32\nK 212,-4096\nZ 63,63,55\nN 136,172,36\nds_read_b128 v[84:87], v9 offset:4096\nC 74,60,61\nN 136,160,40\nC 75,62,63\nRA 29,85\nN 136,164,28\n"
"T 74,19712\nN 140,188,28\nD 28,1,20320\nRA 26,82\nN 140,184,32\nK 196,-3072\nRA 30,86\nN 140,200,36\nRA 27,83\nRA 31,87\nN 140,176,40\nEA 36,24\nN 144,172,40\nEA 37,25\n"
"N 144,168,36\nK 204,-2048\nE3 40,36\nN 144,160,44\nEA 38,26\nN 144,164,32\nEA 39,27\nN 148,188,32\nD 32,0,17664\nE3 41,37\nN 148,184,36\nK 216,-1024\nE3 42,38\nN 148,200,40\n"
"EX 28,28\nN 148,176,44\nEX 29,29\nN 152,172,44\nEX 30,30\nN 152,168,40\nK 236,3072\nE3 43,39\nN 152,160,128\nEX 31,31\nN 152,164,36\nE4 28,28,40\nE4 29,29,41\nN 156,188,36\n"
"D 36,0,17920\nE4 30,30,42\nN 156,184,40\nK 228,0\nE4 31,31,43\nN 156,200,44\nE5 36,40\nE5 37,41\nN 156,176,128\nE5 38,42\nE5 39,43\nN 160,172,128\nRC 28,28\nN 160,168,44\n"
"K 232,1024\nE6 36,24,36\nN 160,160,140\nRC 29,29\nN 160,164,40\nRC 30,30\nN 164,188,40\nD 40,0,18176\nE6 37,25,37\nN 164,184,44\nK 224,2048\nv_add_u32 v15, 0x2000, v15\n"
"N 164,200,128\nRC 31,31\nN 164,176,140\nE6 38,26,38\nE6 39,27,39\nN 168,172,140\nZ 36,36,28\nZ 37,37,29\nN 168,168,128\ns_waitcnt lgkmcnt(15)\nK 244,-4096\nZ 38,38,30\n"
"N 168,160,148\nZ 39,39,31\nC 76,36,37\nN 168,164,44\nC 77,38,39\nRA 48,88\nN 172,188,44\nD 44,0,18432\nRA 52,92\nN 172,184,128\nK 240,-3072\nRA 49,89\nN 172,200,140\n.Lpad17:\n"
".if (.Lpad17 - .Lbase) != 69632\n.error \"pad\"\n.endif\ns_cbranch_execz .Lret2\nRA 53,93\nRA 50,90\nN 172,176,148\nRA 54,94\nRA 51,91\nN 176,172,148\nRA 55,95\nRA 24,96\n"
"N 176,168,140\nK 248,-2048\nRA 28,100\nN 176,160,136\nEA 60,48\nN 176,164,128\nEA 61,49\nN 180,188,128\nD 128,0,18688\nE3 64,60\nN 180,184,140\nK 252,-1024\nE3 65,61\nN 180,200,148\n"
"EA 62,50\nN 180,176,136\nEA 63,51\nN 184,172,136\nEX 52,52\nN 184,168,148\nK 172,3072\nE3 66,62\nN 184,160,144\nEX 53,53\nN 184,164,140\nEX 54,54\nN 188,188,140\nD 140,0,18944\n"
"E3 67,63\nN 188,184,148\nD 148,0,19200\nE4 52,52,64\nN 188,200,136\nD 136,0,19456\nE4 53,53,65\nN 188,176,144\ns_waitcnt vmcnt(21)\nD 144,0,19744\nE4 54,54,66\nN 128,48,152\n"
"s_waitcnt vmcnt(20) lgkmcnt(15)\nK 168,0\nE5 60,64\nN 128,52,156\ns_waitcnt vmcnt(20)\nEX 55,55\nN 128,56,132\ns_waitcnt vmcnt(19)\nE4 55,55,67\nE5 61,65\nN 128,60,0\n"
"s_waitcnt vmcnt(17)\nE5 62,66\nE5 63,67\nN 132,208,0\ns_waitcnt vmcnt(16)\nK 160,1024\nE6 60,48,60\nN 132,180,152\ns_waitcnt vmcnt(16)\nD 152,0,20000\nE6 61,49,61\nN 132,220,156\n"
"s_waitcnt vmcnt(15)\nRC 52,52\nN 132,192,132\nRC 53,53\nN 136,52,132\nK 164,2048\nv_add_u32 v15, 0x6000, v15\nN 136,56,0\nRC 54,54\nN 136,60,4\nRC 55,55\nN 136,48,156\nE6 62,50,62\n"
"E6 63,51,63\nN 140,180,156\nD 156,0,20256\nZ 60,60,52\nN 140,220,132\nK 188,-4096\nZ 61,61,53\nN 140,192,0\nZ 62,62,54\nZ 63,63,55\nN 140,208,4\nC 78,60,61\nC 79,62,63\nN 144,56,4\n"
"RA 25,97\nRA 29,101\nN 144,52,0\nK 184,-3072\nRA 26,98\nN 144,60,8\nRA 30,102\nRA 27,99\nN 144,48,132\nRA 31,103\nN 148,180,132\nD 132,0,17728\nN 148,220,0\nK 200,-2048\n"
"N 148,192,4\nEA 36,24\nN 148,208,8\nEA 37,25\nN 152,56,8\nEA 38,26\nN 152,52,4\nK 176,-1024\nE3 40,36\nN 152,60,12\nEA 39,27\nN 152,48,0\nEX 28,28\nN 156,180,0\nD 0,0,17984\n"
"E3 41,37\nN 156,220,4\ns_waitcnt lgkmcnt(15)\nP 84,50,0\nN 156,192,8\nT 76,21760\nN 156,208,12\nds_read_b128 v[88:91], v9 offset:8192\nE3 42,38\nN 160,56,12\nT 78,23808\nN 160,52,8\n"
"EX 29,29\nN 160,60,16\nEX 30,30\nN 160,48,4\nEX 31,31\nN 164,180,4\nD 4,0,18240\nE3 43,39\nN 164,220,8\nE4 28,28,40\nE4 29,29,41\nN 164,192,12\nE4 30,30,42\nE4 31,31,43\n"
"N 164,208,16\ns_waitcnt lgkmcnt(2)\nP 88,52,0\nN 168,56,16\nE5 36,40\nE5 37,41\nN 168,52,12\nE5 38,42\nE5 39,43\nN 168,60,20\nRC 28,28\nN 168,48,8\nRC 29,29\nN 172,180,8\n"
"D 8,0,18496\nE6 36,24,36\nN 172,220,12\nRC 30,30\nN 172,192,16\nRC 31,31\nN 172,208,20\nE6 37,25,37\nE6 38,26,38\nN 176,56,20\nE6 39,27,39\nZ 36,36,28\nN 176,52,16\nZ 37,37,29\n"
"Z 38,38,30\nN 176,60,24\nZ 39,39,31\nC 72,36,37\nN 176,48,12\nC 73,38,39\nRA 48,104\nN 180,180,12\nD 12,0,18752\nRA 52,108\nN 180,220,16\nT 72,25856\nN 180,192,20\nRA 49,105\n"
"RA 53,109\nN 180,208,24\nRA 50,106\nRA 54,110\nN 184,56,24\nK 56,3072\nRA 51,107\nN 184,52,20\nRA 55,111\nRA 24,112\nN 184,60,28\nEA 60,48\nN 184,48,16\nEA 61,49\nN 188,180,16\n"
"D 16,0,19008\nE3 64,60\nN 188,220,20\nD 20,0,19264\nE3 65,61\nN 188,192,24\nD 24,0,19520\nE5 60,64\nN 188,208,28\ns_waitcnt vmcnt(22)\nD 28,0,19808\nE5 61,65\nN 128,212,32\n"
"s_waitcnt vmcnt(21)\nK 52,0\nE6 60,48,60\nN 128,196,36\ns_waitcnt vmcnt(21)\nEA 62,50\nN 128,204,40\ns_waitcnt vmcnt(20)\nEA 63,51\nN 128,216,44\ns_waitcnt vmcnt(19)\nEX 52,52\n"
"N 132,236,44\ns_waitcnt vmcnt(18)\nK 60,1024\nE3 66,62\nN 132,228,32\ns_waitcnt vmcnt(18)\nD 32,0,20064\nE3 67,63\nN 132,232,36\ns_waitcnt vmcnt(17)\nEX 53,53\nN 132,224,40\n"
"EX 54,54\nN 136,196,40\nK 48,2048\nv_add_u32 v15, 0x2000, v15\nN 136,204,44\nEX 55,55\nN 136,216,128\nE4 52,52,64\nE4 53,53,65\nN 136,212,36\nE4 54,54,66\nE4 55,55,67\nN 140,228,36\n"
"D 36,0,20320\nE5 62,66\nN 140,232,40\nK 180,-4096\nE5 63,67\nN 140,224,44\nds_read_b128 v[92:95], v9 offset:12288\nE6 61,49,61\nN 140,236,128\nRC 52,52\nN 144,204,128\nRC 53,53\n"
"N 144,196,44\nK 220,-3072\nE6 62,50,62\nN 144,216,140\nRC 54,54\nN 144,212,40\nRC 55,55\nN 148,228,40\nD 40,2,17664\nE6 63,51,63\nN 148,232,44\nK 192,-2048\nZ 60,60,52\n"
"N 148,224,128\nZ 61,61,53\nZ 62,62,54\nN 148,236,140\nZ 63,63,55\nC 74,60,61\nN 152,204,140\nC 75,62,63\nRA 28,116\nN 152,196,128\nK 208,-1024\nRA 25,113\nN 152,216,148\nT 74,27904\n"
"N 152,212,44\nRA 29,117\nRA 26,114\nN 156,228,44\nD 44,2,17920\nRA 30,118\nN 156,232,128\nRA 27,115\nRA 31,119\nN 156,224,140\nEA 36,24\nN 156,236,148\nEA 37,25\nN 160,204,148\n"
"EA 38,26\nN 160,196,140\nEA 39,27\nN 160,216,136\nEX 28,28\nN 160,212,128\nEX 29,29\nN 164,228,128\nD 128,2,18176\nE3 40,36\nN 164,232,140\nEX 30,30\nN 164,224,148\n"
"s_waitcnt lgkmcnt(4)\nP 92,54,0\nN 164,236,136\nEX 31,31\nN 168,204,136\nE3 41,37\nE3 42,38\nN 168,196,148\nE3 43,39\nE4 28,28,40\nN 168,216,144\nE4 29,29,41\nE4 30,30,42\n"
"N 168,212,140\nE4 31,31,43\nE5 36,40\nN 172,228,140\nD 140,2,18432\nE5 37,41\nN 172,232,148\nE5 38,42\nE5 39,43\nN 172,224,136\nRC 28,28\nN 172,236,144\nRC 29,29\nN 176,204,144\n"
"RC 30,30\nN 176,196,136\nRC 31,31\nN 176,216,152\nE6 36,24,36\nE6 37,25,37\nN 176,212,148\nE6 38,26,38\nE6 39,27,39\nN 180,228,148\nD 148,2,18688\nZ 36,36,28\nN 180,232,136\n"
"Z 37,37,29\nZ 38,38,30\nN 180,224,144\nZ 39,39,31\nC 76,36,37\nN 180,236,152\nC 77,38,39\nRA 48,120\nN 184,204,152\nK 204,3072\nRA 52,124\nN 184,196,144\nT 76,29952\nN 184,216,156\n"
"RA 49,121\nRA 53,125\nN 184,212,136\nRA 50,122\nRA 54,126\nN 188,228,136\nD 136,2,18944\nRA 51,123\nN 188,232,144\nD 144,2,19200\nRA 55,127\nN 188,224,152\nD 152,2,19456\n"
"N 188,236,156\ns_waitcnt vmcnt(23)\nD 156,2,19744\nN 128,244,132\ns_waitcnt vmcnt(22)\nK 196,0\nN 128,240,0\ns_waitcnt vmcnt(22)\nEA 60,48\nN 128,248,4\ns_waitcnt vmcnt(21)\n"
"EA 61,49\nN 128,252,8\ns_waitcnt vmcnt(20)\nEA 62,50\nN 132,172,8\ns_waitcnt vmcnt(19)\nK 216,1024\nE3 64,60\nN 132,168,132\ns_waitcnt vmcnt(19)\nD 132,2,20000\nE3 65,61\n"
"N 132,160,0\ns_waitcnt vmcnt(18)\nEA 63,51\nN 132,164,4\nEX 52,52\nN 136,240,4\nK 212,2048\nv_add_u32 v15, 0x2000, v15\nN 136,248,8\nEX 53,53\nN 136,252,12\nEX 54,54\nN 136,244,0\n"
"EX 55,55\nN 140,168,0\nD 0,2,20256\nE3 66,62\nN 140,160,4\nK 228,-4096\nE3 67,63\nN 140,164,8\nE4 52,52,64\nE4 53,53,65\nN 140,172,12\nE4 54,54,66\nE4 55,55,67\nN 144,248,12\n"
"E5 60,64\nE5 61,65\nN 144,240,8\nK 232,-3072\nE5 62,66\nN 144,252,16\nE5 63,67\nE6 60,48,60\nN 144,244,4\nRC 52,52\nN 148,168,4\nD 4,2,17728\nE6 61,49,61\nN 148,160,8\nK 224,-2048\n"
"E6 62,50,62\nN 148,164,12\nRC 53,53\nN 148,172,16\nRC 54,54\nN 152,248,16\nRC 55,55\nN 152,240,12\nK 236,-1024\nv_add_u32 v15, 0x2000, v15\nN 152,252,20\nE6 63,51,63\nZ 60,60,52\n"
"N 152,244,8\nZ 61,61,53\nZ 62,62,54\nN 156,168,8\nD 8,2,17984\nZ 63,63,55\nN 156,160,12\nK 64,-4096\nC 78,60,61\nN 156,164,16\nC 79,62,63\nN 156,172,20\nT 78,32000\nN 160,248,20\n"
"EA 36,128\nN 160,240,16\nK 68,-3072\nE3 40,36\nN 160,252,24\nEA 37,129\nN 160,244,12\nEA 38,130\nN 164,168,12\nD 12,2,18240\nE3 41,37\nN 164,160,16\nK 72,-2048\nE3 42,38\n"
"N 164,164,20\nEA 39,131\nN 164,172,24\nEX 28,132\nN 168,248,24\ns_waitcnt lgkmcnt(1)\ns_barrier\nE3 43,39\nN 168,240,20\nK 76,-1024\nv_add_u32 v15, 0x2000, v15\nN 168,252,28\n"
"ds_read_b128 v[80:83], v9 offset:17664\nE4 28,28,40\nN 168,244,16\nEX 29,133\nN 172,168,16\nD 16,2,18496\nE4 29,29,41\nN 172,160,20\nK 80,-4096\nE5 36,40\nN 172,164,24\nEX 30,134\n"
"N 172,172,28\nEX 31,135\nN 176,248,28\nE4 30,30,42\nE4 31,31,43\nN 176,240,24\nK 84,-3072\nE5 37,41\nN 176,252,32\nE5 38,42\nE5 39,43\nN 176,244,20\nRC 28,28\nN 180,168,20\n"
"D 20,2,18752\nE6 36,128,36\nN 180,160,24\nK 88,-2048\nE6 37,129,37\nN 180,164,28\nRC 29,29\nN 180,172,32\nRC 30,30\nN 184,248,32\nRC 31,31\nN 184,240,28\nK 92,-1024\n"
"v_add_u32 v15, 0x2000, v15\nN 184,252,36\nE6 38,130,38\nE6 39,131,39\nN 184,244,24\nZ 36,36,28\nZ 37,37,29\nN 188,168,24\nD 24,2,19008\nZ 38,38,30\nN 188,160,28\nD 28,2,19264\n"
"Z 39,39,31\nN 188,164,32\nD 32,2,19520\nC 72,36,37\nN 188,172,36\ns_waitcnt vmcnt(30)\nD 36,2,19808\nC 73,38,39\nN 192,188,40\ns_waitcnt vmcnt(29)\nK 96,-4096\nN 192,184,44\n"
"s_waitcnt vmcnt(29)\nEA 60,136\nN 192,200,128\n.Lpad18:\n.if (.Lpad18 - .Lbase) != 73740\n.error \"pad\"\n.endif\ns_cbranch_execz .Lret2\ns_waitcnt vmcnt(28)\nEA 61,137\n"
"N 192,176,140\ns_waitcnt vmcnt(25)\nEA 62,138\nN 196,56,140\ns_waitcnt vmcnt(24)\nK 100,-3072\nE3 64,60\nN 196,52,40\ns_waitcnt vmcnt(24)\nD 40,2,20064\nE3 65,61\nN 196,60,44\n"
"s_waitcnt vmcnt(23)\nEA 63,139\nN 196,48,128\nEX 52,140\nN 200,184,128\nK 104,-2048\nE3 66,62\nN 200,200,140\nEX 53,141\nN 200,176,148\nEX 54,142\nN 200,188,44\nEX 55,143\n"
"N 204,52,44\nD 44,2,20320\nE3 67,63\nN 204,60,128\nK 108,-1024\nE4 52,52,64\nN 204,48,140\nE4 53,53,65\nE4 54,54,66\nN 204,56,148\nE4 55,55,67\nE5 60,64\nN 208,200,148\nE5 61,65\n"
"E5 62,66\nN 208,184,140\nK 112,3072\nE5 63,67\nN 208,176,136\nRC 52,52\nN 208,188,128\nRC 53,53\nN 212,52,128\nD 128,2,0\nE6 60,136,60\nN 212,60,140\nK 116,2048\nE6 61,137,61\n"
"N 212,48,148\nRC 54,54\nN 212,56,136\nRC 55,55\nN 216,200,136\nE6 62,138,62\nE6 63,139,63\nN 216,184,148\nK 120,1024\nZ 60,60,52\nN 216,176,144\nZ 61,61,53\nZ 62,62,54\n"
"N 216,188,140\nZ 63,63,55\nC 74,60,61\nN 220,52,140\nD 140,2,256\nC 75,62,63\nN 220,60,148\nK 124,0\nv_add_u32 v15, 0xffffe000, v15\nN 220,48,136\nEA 36,144\nN 220,56,144\n"
"EA 37,145\nN 224,200,144\nEA 38,146\nN 224,184,136\nK 248,3072\nE3 40,36\nN 224,176,152\nEA 39,147\nN 224,188,148\nEX 28,148\nN 228,52,148\nD 148,2,512\nE3 41,37\nN 228,60,136\n"
"K 240,2048\nE3 42,38\nN 228,48,144\nEX 29,149\nN 228,56,152\nEX 30,150\nN 232,200,152\nEX 31,151\nN 232,184,144\nK 252,1024\nE3 43,39\nN 232,176,156\nE4 28,28,40\nE4 29,29,41\n"
"N 232,188,136\nE4 30,30,42\nE4 31,31,43\nN 236,52,136\nD 136,2,768\nE5 36,40\nN 236,60,144\nK 244,0\nv_add_u32 v15, 0xffffe000, v15\nN 236,48,152\nE5 37,41\nE5 38,42\nN 236,56,156\n"
"E5 39,43\nE6 36,144,36\nN 240,200,156\nRC 28,28\nN 240,184,152\nK 168,3072\nE6 37,145,37\nN 240,176,132\nRC 29,29\nN 240,188,144\nRC 30,30\nN 244,52,144\nD 144,2,64\nE6 38,146,38\n"
"N 244,60,152\nK 160,2048\nE6 39,147,39\nN 244,48,156\nRC 31,31\nN 244,56,132\nZ 36,36,28\nZ 37,37,29\nN 248,200,132\nZ 38,38,30\nZ 39,39,31\nN 248,184,156\nK 164,1024\nC 76,36,37\n"
"N 248,176,0\nC 77,38,39\nN 248,188,152\nEA 60,152\nN 252,52,152\nD 152,2,320\nE3 64,60\nN 252,60,156\nD 156,2,576\nE5 60,64\nN 252,48,132\nD 132,2,832\nE6 60,152,60\nN 252,56,0\n"
"s_waitcnt vmcnt(35)\nD 0,2,35328\nN 192,180,4\ns_waitcnt vmcnt(34)\nK 172,0\nv_add_u32 v15, 0xffffe000, v15\nN 192,220,8\ns_waitcnt vmcnt(34) lgkmcnt(15)\nEA 61,153\nN 192,192,12\n"
"s_waitcnt vmcnt(33)\nEA 62,154\nN 192,208,16\ns_waitcnt vmcnt(31)\nEA 63,155\nN 196,204,16\ns_waitcnt vmcnt(30)\nK 200,3072\nE3 65,61\nN 196,196,4\ns_waitcnt vmcnt(30)\nD 4,2,35584\n"
"E3 66,62\nN 196,216,8\ns_waitcnt vmcnt(29)\nEX 52,156\nN 196,212,12\nEX 53,157\nN 200,220,12\nK 184,2048\nE3 67,63\nN 200,192,16\nEX 54,158\nN 200,208,20\nEX 55,159\nN 200,180,8\n"
"E4 52,52,64\nE4 53,53,65\nN 204,196,8\nD 8,2,35840\nE4 54,54,66\nN 204,216,12\nK 176,0\nE4 55,55,67\nN 204,212,16\nE5 61,65\nE5 62,66\nN 204,204,20\nE5 63,67\nE6 61,153,61\n"
"N 208,192,20\nRC 52,52\nN 208,220,16\ns_waitcnt lgkmcnt(15)\nK 188,1024\nE6 62,154,62\nN 208,208,24\nRC 53,53\nN 208,180,12\nRC 54,54\nN 212,196,12\nD 12,2,36096\nE6 63,155,63\n"
"N 212,216,16\nP 80,48,3072\nN 212,212,20\nT 72,35328\nN 212,204,24\nds_read_b128 v[84:87], v9 offset:21760\nZ 60,60,52\nN 216,192,24\nT 74,37376\nN 216,220,20\nRC 55,55\n"
"N 216,208,28\nZ 61,61,53\nZ 62,62,54\nN 216,180,16\nZ 63,63,55\nC 78,60,61\nN 220,196,16\nD 16,2,35392\nC 79,62,63\nN 220,216,20\nEA 36,160\nN 220,212,24\nEA 37,161\nN 220,204,28\n"
"s_waitcnt lgkmcnt(2)\nP 84,50,3072\nN 224,192,28\nT 76,39424\nN 224,220,24\nds_read_b128 v[88:91], v9 offset:25856\nE3 40,36\nN 224,208,32\nT 78,41472\nN 224,180,20\nEA 38,162\n"
"N 228,196,20\nD 20,2,35648\nE3 41,37\nN 228,216,24\nEA 39,163\nN 228,212,28\nEX 28,164\nN 228,204,32\nEX 29,165\nN 232,192,32\nEX 30,166\nN 232,220,28\nEX 31,167\nN 232,208,36\n"
"E3 42,38\nE3 43,39\nN 232,180,24\nE4 28,28,40\nE4 29,29,41\nN 236,196,24\nD 24,2,35904\nE4 30,30,42\nN 236,216,28\nE4 31,31,43\nE5 36,40\nN 236,212,32\nE5 37,41\nE5 38,42\n"
"N 236,204,36\ns_waitcnt lgkmcnt(3)\nP 88,52,3072\nN 240,192,36\nE5 39,43\nE6 36,160,36\nN 240,220,32\nRC 28,28\nN 240,208,40\nRC 29,29\nN 240,180,28\nRC 30,30\nN 244,196,28\n"
"D 28,2,36160\nE6 37,161,37\nN 244,216,32\nRC 31,31\nN 244,212,36\nE6 38,162,38\nE6 39,163,39\nN 244,204,40\nZ 36,36,28\nZ 37,37,29\nN 248,192,40\nZ 38,38,30\nZ 39,39,31\n"
"N 248,220,36\nC 72,36,37\nC 73,38,39\nN 248,208,44\nT 72,43520\nN 248,180,32\nEA 60,168\nN 252,196,32\nD 32,2,1024\nE3 64,60\nN 252,216,36\nD 36,2,1088\nE5 60,64\nN 252,212,40\n"
"D 40,2,36352\nE6 60,168,60\nN 252,204,44\ns_waitcnt vmcnt(34)\nD 44,2,36416\nN 192,228,128\ns_waitcnt vmcnt(33)\nEA 61,169\nN 192,232,140\ns_waitcnt vmcnt(32)\nEA 62,170\n"
"N 192,224,148\ns_waitcnt vmcnt(31)\nEA 63,171\nN 192,236,136\ns_waitcnt vmcnt(30)\nEX 52,172\nN 192,64,144\ns_waitcnt vmcnt(29)\nEX 53,173\nN 192,68,152\ns_waitcnt vmcnt(28)\n"
"EX 54,174\nN 192,72,156\ns_waitcnt vmcnt(27)\nEX 55,175\nN 192,76,132\ns_waitcnt vmcnt(26)\nE3 65,61\nE3 66,62\nN 192,80,0\ns_waitcnt vmcnt(25)\nE3 67,63\nE4 52,52,64\nN 192,84,4\n"
"s_waitcnt vmcnt(24)\nE4 53,53,65\nE4 54,54,66\nN 192,88,8\ns_waitcnt vmcnt(23)\nE4 55,55,67\nE5 61,65\nN 192,92,12\ns_waitcnt vmcnt(22)\nE5 62,66\nE5 63,67\nN 192,96,16\n"
"s_waitcnt vmcnt(21) lgkmcnt(7)\nds_read_b128 v[92:95], v9 offset:29952\nE6 61,169,61\nN 192,100,20\ns_waitcnt vmcnt(20) lgkmcnt(7)\nRC 52,52\nN 192,104,24\n"
"s_waitcnt vmcnt(19) lgkmcnt(6)\nRC 53,53\nN 192,108,28\ns_waitcnt vmcnt(18)\nRC 54,54\nN 196,112,28\ns_waitcnt vmcnt(17)\nRC 55,55\nN 196,116,24\ns_waitcnt vmcnt(16)\nE6 62,170,62\n"
"E6 63,171,63\nN 196,120,20\ns_waitcnt vmcnt(15)\nZ 60,60,52\nZ 61,61,53\nN 196,124,16\ns_waitcnt vmcnt(14)\nD 16,2,1280\nZ 62,62,54\nN 196,248,12\ns_waitcnt vmcnt(13)\nZ 63,63,55\n"
"C 74,60,61\nN 196,240,8\ns_waitcnt vmcnt(12)\nC 75,62,63\nN 196,252,4\ns_waitcnt vmcnt(11)\nT 74,45568\nN 196,244,0\ns_waitcnt vmcnt(10)\nD 0,2,1344\nN 196,168,132\n"
"s_waitcnt vmcnt(9)\nEA 36,176\nN 196,160,156\ns_waitcnt vmcnt(8)\nEA 37,177\nN 196,164,152\ns_waitcnt vmcnt(7)\nEA 38,178\nN 196,172,144\ns_waitcnt vmcnt(6)\nD 144,2,36608\n"
"E3 40,36\nN 196,200,136\ns_waitcnt vmcnt(5)\nEA 39,179\nN 196,184,148\ns_waitcnt vmcnt(4)\nEX 28,180\nN 196,176,128\ns_waitcnt vmcnt(3)\nD 128,2,36672\nE3 41,37\nN 196,188,140\n"
"EX 29,181\nN 200,228,140\nEX 30,182\nN 200,64,152\nEX 31,183\nN 200,80,4\nE3 42,38\nE3 43,39\nN 200,96,20\ns_waitcnt lgkmcnt(5)\nP 92,54,3072\nN 200,232,148\nE4 28,28,40\n"
"E4 29,29,41\nN 200,68,156\nE4 30,30,42\nE4 31,31,43\nN 200,84,8\nE5 36,40\nE5 37,41\nN 200,100,24\nE5 38,42\nE5 39,43\nN 200,224,136\nRC 28,28\nN 200,72,132\nRC 29,29\nN 200,88,12\n"
"RC 30,30\nN 200,104,28\nRC 31,31\nN 200,236,32\nE6 36,176,36\nE6 37,177,37\nN 200,76,36\nE6 38,178,38\nE6 39,179,39\nN 200,92,40\nZ 36,36,28\nZ 37,37,29\nN 200,108,44\nZ 38,38,30\n"
"Z 39,39,31\nN 204,112,44\nC 76,36,37\nC 77,38,39\nN 204,248,40\nT 76,47616\nN 204,168,36\nEA 60,184\nN 204,200,32\nEA 61,185\nN 204,116,28\nEA 62,186\nN 204,240,12\nEA 63,187\n"
"N 204,160,132\nEX 52,188\nN 204,184,136\nEX 53,189\nN 204,120,24\nEX 54,190\nN 204,252,8\nEX 55,191\nN 204,164,156\nE3 64,60\nE3 65,61\nN 204,124,20\nD 20,2,1536\nE3 66,62\n"
"N 204,244,4\nD 4,2,1600\nE3 67,63\nN 204,172,152\nD 152,2,36864\nE4 52,52,64\nN 204,176,140\nD 140,2,36928\nE4 53,53,65\nN 204,188,148\nE4 54,54,66\nE4 55,55,67\nN 208,228,148\n"
"E5 60,64\nE5 61,65\nN 208,64,156\nE5 62,66\nE5 63,67\nN 208,80,8\nRC 52,52\nN 208,96,24\nRC 53,53\nN 208,232,136\nRC 54,54\nN 208,68,132\nRC 55,55\nN 208,84,12\nE6 60,184,60\n"
"E6 61,185,61\nN 208,100,28\nE6 62,186,62\nE6 63,187,63\nN 208,224,32\nZ 60,60,52\nZ 61,61,53\nN 208,72,36\nZ 62,62,54\nZ 63,63,55\nN 208,88,40\nC 78,60,61\nC 79,62,63\nN 208,104,44\n"
"s_waitcnt lgkmcnt(9)\nT 78,49664\nN 208,236,16\ns_waitcnt lgkmcnt(8)\nEA 36,192\nN 208,76,0\ns_waitcnt lgkmcnt(7)\nEA 37,193\nN 208,92,144\ns_waitcnt lgkmcnt(6)\nEA 38,194\n"
"N 208,108,128\nEA 39,195\nN 212,112,128\nEX 28,196\nN 212,248,144\ns_waitcnt lgkmcnt(0)\ns_barrier\nE3 40,36\nN 212,168,0\nds_read_b128 v[80:83], v9 offset:35328\nE3 41,37\n"
"N 212,200,16\nEX 29,197\nN 212,116,44\nEX 30,198\nN 212,240,40\nEX 31,199\nN 212,160,36\nE3 42,38\nE3 43,39\nN 212,184,32\nE4 28,28,40\nE4 29,29,41\nN 212,120,28\nE4 30,30,42\n"
"E4 31,31,43\nN 212,252,12\nE5 36,40\nE5 37,41\nN 212,164,132\nE5 38,42\nE5 39,43\nN 212,124,24\nD 24,2,1792\nE6 36,192,36\nN 212,244,8\n.Lpad19:\n.if (.Lpad19 - .Lbase) != 77844\n"
".error \"pad\"\n.endif\ns_cbranch_execz .Lret2\nD 8,2,1856\nE6 37,193,37\nN 212,172,156\nD 156,2,37120\nE6 38,194,38\nN 212,176,148\nD 148,2,37184\nE6 39,195,39\nN 212,188,136\n"
"RC 28,28\nN 216,228,136\nRC 29,29\nN 216,64,132\nRC 30,30\nN 216,80,12\nRC 31,31\nN 216,96,28\nZ 36,36,28\nZ 37,37,29\nN 216,232,32\nZ 38,38,30\nZ 39,39,31\nN 216,68,36\nC 72,36,37\n"
"C 73,38,39\nN 216,84,40\nds_read_b128 v[84:87], v9 offset:39424\nN 216,100,44\ns_waitcnt lgkmcnt(5)\nP 80,48,1024\nN 216,224,16\nT 72,0\nN 216,72,0\nEA 60,200\nN 216,88,144\n"
"EA 61,201\nN 216,104,128\nEA 62,202\nN 216,236,20\nEA 63,203\nN 216,76,4\nEX 52,204\nN 216,92,152\nEX 53,205\nN 216,108,140\nEX 54,206\nN 220,112,140\nEX 55,207\nN 220,248,152\n"
"E3 64,60\nE3 65,61\nN 220,168,4\nE3 66,62\nE3 67,63\nN 220,200,20\nE4 52,52,64\nE4 53,53,65\nN 220,116,128\nE4 54,54,66\nE4 55,55,67\nN 220,240,144\nE5 60,64\nE5 61,65\nN 220,160,0\n"
"E5 62,66\nE5 63,67\nN 220,184,16\nRC 52,52\nN 220,120,44\nRC 53,53\nN 220,252,40\nRC 54,54\nN 220,164,36\nRC 55,55\nN 220,124,28\nD 28,2,2080\nE6 60,200,60\nN 220,244,12\n"
"D 12,2,2144\nE6 61,201,61\nN 220,172,132\nD 132,2,37408\nE6 62,202,62\nN 220,176,136\nD 136,2,37472\nE6 63,203,63\nN 220,188,32\ns_waitcnt lgkmcnt(5)\nP 84,50,1024\nN 224,228,32\n"
"ds_read_b128 v[88:91], v9 offset:43520\nZ 60,60,52\nN 224,64,36\nZ 61,61,53\nZ 62,62,54\nN 224,80,40\nZ 63,63,55\nC 74,60,61\nN 224,96,44\nC 75,62,63\nN 224,232,16\nT 74,2048\n"
"N 224,68,0\nEA 36,208\nN 224,84,144\nEA 37,209\nN 224,100,128\nEA 38,210\nN 224,224,20\nEA 39,211\nN 224,72,4\nEX 28,212\nN 224,88,152\nEX 29,213\nN 224,104,140\nEX 30,214\n"
"N 224,236,24\nEX 31,215\nN 224,76,8\nE3 40,36\nE3 41,37\nN 224,92,156\nE3 42,38\nE3 43,39\nN 224,108,148\nE4 28,28,40\nE4 29,29,41\nN 228,112,148\nE4 30,30,42\nE4 31,31,43\n"
"N 228,248,156\nE5 36,40\nE5 37,41\nN 228,168,8\nE5 38,42\nE5 39,43\nN 228,200,24\ns_waitcnt lgkmcnt(1)\nP 88,52,1024\nN 228,116,140\nds_read_b128 v[92:95], v9 offset:47616\n"
"E6 36,208,36\nN 228,240,152\nRC 28,28\nN 228,160,4\nRC 29,29\nN 228,184,20\nRC 30,30\nN 228,120,128\nRC 31,31\nN 228,252,144\nE6 37,209,37\nE6 38,210,38\nN 228,164,0\nE6 39,211,39\n"
"Z 36,36,28\nN 228,124,44\nD 44,2,2336\nZ 37,37,29\nN 228,244,40\nD 40,2,2400\nZ 38,38,30\nN 228,172,36\nD 36,2,37664\nZ 39,39,31\nN 228,176,32\nD 32,2,37728\nC 76,36,37\n"
"N 228,188,16\nC 77,38,39\nN 232,228,16\nT 76,4096\nN 232,64,0\nEA 60,216\nN 232,80,144\nEA 61,217\nN 232,96,128\nEA 62,218\nN 232,232,20\nEA 63,219\nN 232,68,4\nEX 52,220\n"
"N 232,84,152\nEX 53,221\nN 232,100,140\nEX 54,222\nN 232,224,24\nEX 55,223\nN 232,72,8\nE3 64,60\nE3 65,61\nN 232,88,156\nE3 66,62\nE3 67,63\nN 232,104,148\ns_waitcnt lgkmcnt(5)\n"
"P 92,54,1024\nN 232,236,28\nE4 52,52,64\nE4 53,53,65\nN 232,76,12\nE4 54,54,66\nE4 55,55,67\nN 232,92,132\nE5 60,64\nE5 61,65\nN 232,108,136\nE5 62,66\nE5 63,67\nN 236,112,136\n"
"RC 52,52\nN 236,248,132\nRC 53,53\nN 236,168,12\nRC 54,54\nN 236,200,28\nRC 55,55\nN 236,116,148\nE6 60,216,60\nE6 61,217,61\nN 236,240,156\nE6 62,218,62\nE6 63,219,63\nN 236,160,8\n"
"Z 60,60,52\nZ 61,61,53\nN 236,184,24\nZ 62,62,54\nZ 63,63,55\nN 236,120,140\nC 78,60,61\nC 79,62,63\nN 236,252,152\nT 78,6144\nN 236,164,4\nEA 36,224\nN 236,124,128\nD 128,2,2592\n"
"E3 40,36\nN 236,244,144\nD 144,2,2656\nE5 36,40\nN 236,172,0\nD 0,2,37920\nE6 36,224,36\nN 236,176,16\nD 16,2,37984\nN 236,188,20\nEA 37,225\nN 240,228,20\nEA 38,226\nN 240,64,4\n"
"EA 39,227\nN 240,80,152\nEX 28,228\nN 240,96,140\ns_waitcnt lgkmcnt(4)\ns_barrier\nE3 41,37\nN 240,232,24\nds_read_b128 v[80:83], v9 offset:0\nE3 42,38\nN 240,68,8\nEX 29,229\n"
"N 240,84,156\nEX 30,230\nN 240,100,148\nEX 31,231\nN 240,224,28\nE3 43,39\nE4 28,28,40\nN 240,72,12\nE4 29,29,41\nE4 30,30,42\nN 240,88,132\nE4 31,31,43\nE5 37,41\nN 240,104,136\n"
"E5 38,42\nE5 39,43\nN 240,236,44\nRC 28,28\nN 240,76,40\nRC 29,29\nN 240,92,36\nRC 30,30\nN 240,108,32\nRC 31,31\nN 244,112,32\nE6 37,225,37\nE6 38,226,38\nN 244,248,36\n"
"ds_read_b128 v[84:87], v9 offset:4096\nE6 39,227,39\nN 244,168,40\nZ 36,36,28\nZ 37,37,29\nN 244,200,44\nZ 38,38,30\nZ 39,39,31\nN 244,116,136\nC 72,36,37\nC 73,38,39\n"
"N 244,240,132\ns_waitcnt lgkmcnt(1)\nP 80,48,2048\nN 244,160,12\nT 72,8192\nN 244,184,28\nEA 60,232\nN 244,120,148\nEA 61,233\nN 244,252,156\nEA 62,234\nN 244,164,8\nEA 63,235\n"
"N 244,124,140\nEX 52,236\nN 244,244,152\nEX 53,237\nN 244,172,4\nEX 54,238\nN 244,176,20\nEX 55,239\nN 244,188,24\nE3 64,60\nE3 65,61\nN 248,228,24\nE3 66,62\nE3 67,63\nN 248,64,8\n"
"E4 52,52,64\nE4 53,53,65\nN 248,80,156\nE4 54,54,66\nE4 55,55,67\nN 248,96,148\nE5 60,64\nE5 61,65\nN 248,232,28\nE5 62,66\nE5 63,67\nN 248,68,12\ns_waitcnt lgkmcnt(1)\n"
"P 84,50,2048\nN 248,84,132\nRC 52,52\nN 248,100,136\nRC 53,53\nN 248,224,44\nRC 54,54\nN 248,72,40\nRC 55,55\nN 248,88,36\nE6 60,232,60\nE6 61,233,61\nN 248,104,32\nE6 62,234,62\n"
"E6 63,235,63\nN 248,236,128\nZ 60,60,52\nZ 61,61,53\nN 248,76,144\nZ 62,62,54\nZ 63,63,55\nN 248,92,0\nC 74,60,61\nC 75,62,63\nN 248,108,16\nT 74,10240\nN 252,112,16\nEA 36,240\n"
"N 252,248,0\nEA 37,241\nN 252,168,144\nEA 38,242\nN 252,200,128\nEA 39,243\nN 252,116,32\nEX 28,244\nN 252,240,36\ns_waitcnt lgkmcnt(0)\ns_barrier\nE3 40,36\nN 252,160,40\n"
"ds_read_b128 v[88:91], v9 offset:8192\nE3 41,37\nN 252,184,44\nEX 29,245\nN 252,120,136\nEX 30,246\nN 252,252,132\nEX 31,247\nN 252,164,12\nE3 42,38\nE3 43,39\nN 252,124,148\n"
"E4 28,28,40\nE4 29,29,41\nN 252,244,156\nE4 30,30,42\nE4 31,31,43\nN 252,172,8\nE5 36,40\nE5 37,41\nN 252,176,24\nE5 38,42\nE5 39,43\nN 252,188,28\nRC 28,28\nRC 29,29\nRC 30,30\n"
"RC 31,31\nE6 36,240,36\nE6 37,241,37\nE6 38,242,38\nE6 39,243,39\nZ 36,36,28\nZ 37,37,29\nZ 38,38,30\nZ 39,39,31\nC 76,36,37\ns_waitcnt lgkmcnt(0)\nP 88,52,2048\nC 77,38,39\n"
"T 76,12288\nEA 60,248\nEA 61,249\nEA 62,250\nEA 63,251\nEX 52,252\nEX 53,253\nEX 54,254\nEX 55,255\nQ3 64,60\nQ3 66,62\nQ4 52,52,64\nQ4 54,54,66\nQ5 60,64\nQ5 62,66\nRC 52,52\n"
"RC 53,53\nRC 54,54\nRC 55,55\nE6 60,248,60\nE6 61,249,61\nE6 62,250,62\nE6 63,251,63\nQZ 60,60,52\nQZ 62,62,54\nC 78,60,61\nC 79,62,63\nT 78,14336\ns_waitcnt lgkmcnt(0)\ns_barrier\n"
"ds_read_b128 v[92:95], v9 offset:12288\ns_waitcnt lgkmcnt(0)\nP 92,54,2048\n"
".Lconv_code_end:\n.purgem M\n.purgem MZ\n.purgem MC\n.purgem N\n.purgem NZ\n.purgem D\n.purgem K\n.purgem G\n.purgem H\n.purgem S\n.purgem S2\n.purgem U\n.purgem L\n.purgem A\n.purgem X\n.purgem Y\n.purgem Z\n.purgem C\n.purgem RA\n.purgem WA\n.purgem E1\n.purgem E2\n.purgem EX\n.purgem EA\n.purgem RC\n.purgem E3\n.purgem E4\n.purgem E5\n.purgem E6\n.purgem Q1\n.purgem Q2\n.purgem Q3\n.purgem Q4\n.purgem Q5\n.purgem QZ\n.purgem T\n.purgem P\n"

    :: "s"(__builtin_amdgcn_kernarg_segment_ptr()), "s"(blockIdx.x), "s"(__builtin_amdgcn_readfirstlane(threadIdx.x)), "s"((unsigned)(size_t)lds)
    : "v0", "v1", "v2", "v3", "v4", "v5", "v6", "v7", "v8", "v9", "v10", "v11", "v12", "v13", "v14", "v15", "v16", "v17", "v18", "v19", "v20", "v21", "v22", "v23", "v24",
    "v25", "v26", "v27", "v28", "v29", "v30", "v31", "v32", "v33", "v34", "v35", "v36", "v37", "v38", "v39", "v40", "v41", "v42", "v43", "v44", "v45", "v46", "v47", "v48",
    "v49", "v50", "v51", "v52", "v53", "v54", "v55", "v56", "v57", "v58", "v59", "v60", "v61", "v62", "v63", "v64", "v65", "v66", "v67", "v68", "v69", "v70", "v71", "v72",
    "v73", "v74", "v75", "v76", "v77", "v78", "v79", "v80", "v81", "v82", "v83", "v84", "v85", "v86", "v87", "v88", "v89", "v90", "v91", "v92", "v93", "v94", "v95", "v96",
    "v97", "v98", "v99", "v100", "v101", "v102", "v103", "v104", "v105", "v106", "v107", "v108", "v109", "v110", "v111", "v112", "v113", "v114", "v115", "v116", "v117",
    "v118", "v119", "v120", "v121", "v122", "v123", "v124", "v125", "v126", "v127", "v128", "v129", "v130", "v131", "v132", "v133", "v134", "v135", "v136", "v137", "v138",
    "v139", "v140", "v141", "v142", "v143", "v144", "v145", "v146", "v147", "v148", "v149", "v150", "v151", "v152", "v153", "v154", "v155", "v156", "v157", "v158", "v159",
    "v160", "v161", "v162", "v163", "v164", "v165", "v166", "v167", "v168", "v169", "v170", "v171", "v172", "v173", "v174", "v175", "v176", "v177", "v178", "v179", "v180",
    "v181", "v182", "v183", "v184", "v185", "v186", "v187", "v188", "v189", "v190", "v191", "v192", "v193", "v194", "v195", "v196", "v197", "v198", "v199", "v200", "v201",
    "v202", "v203", "v204", "v205", "v206", "v207", "v208", "v209", "v210", "v211", "v212", "v213", "v214", "v215", "v216", "v217", "v218", "v219", "v220", "v221", "v222",
    "v223", "v224", "v225", "v226", "v227", "v228", "v229", "v230", "v231", "v232", "v233", "v234", "v235", "v236", "v237", "v238", "v239", "v240", "v241", "v242", "v243",
    "v244", "v245", "v246", "v247", "v248", "v249", "v250", "v251", "v252", "v253", "v254", "v255", "a0", "a1", "a2", "a3", "a4", "a5", "a6", "a7", "a8", "a9", "a10", "a11",
    "a12", "a13", "a14", "a15", "a16", "a17", "a18", "a19", "a20", "a21", "a22", "a23", "a24", "a25", "a26", "a27", "a28", "a29", "a30", "a31", "a32", "a33", "a34", "a35",
    "a36", "a37", "a38", "a39", "a40", "a41", "a42", "a43", "a44", "a45", "a46", "a47", "a48", "a49", "a50", "a51", "a52", "a53", "a54", "a55", "a56", "a57", "a58", "a59",
    "a60", "a61", "a62", "a63", "a64", "a65", "a66", "a67", "a68", "a69", "a70", "a71", "a72", "a73", "a74", "a75", "a76", "a77", "a78", "a79", "a80", "a81", "a82", "a83",
    "a84", "a85", "a86", "a87", "a88", "a89", "a90", "a91", "a92", "a93", "a94", "a95", "a96", "a97", "a98", "a99", "a100", "a101", "a102", "a103", "a104", "a105", "a106",
    "a107", "a108", "a109", "a110", "a111", "a112", "a113", "a114", "a115", "a116", "a117", "a118", "a119", "a120", "a121", "a122", "a123", "a124", "a125", "a126", "a127",
    "a128", "a129", "a130", "a131", "a132", "a133", "a134", "a135", "a136", "a137", "a138", "a139", "a140", "a141", "a142", "a143", "a144", "a145", "a146", "a147", "a148",
    "a149", "a150", "a151", "a152", "a153", "a154", "a155", "a156", "a157", "a158", "a159", "a160", "a161", "a162", "a163", "a164", "a165", "a166", "a167", "a168", "a169",
    "a170", "a171", "a172", "a173", "a174", "a175", "a176", "a177", "a178", "a179", "a180", "a181", "a182", "a183", "a184", "a185", "a186", "a187", "a188", "a189", "a190",
    "a191", "a192", "a193", "a194", "a195", "a196", "a197", "a198", "a199", "a200", "a201", "a202", "a203", "a204", "a205", "a206", "a207", "a208", "a209", "a210", "a211",
    "a212", "a213", "a214", "a215", "a216", "a217", "a218", "a219", "a220", "a221", "a222", "a223", "a224", "a225", "a226", "a227", "a228", "a229", "a230", "a231", "a232",
    "a233", "a234", "a235", "a236", "a237", "a238", "a239", "a240", "a241", "a242", "a243", "a244", "a245", "a246", "a247", "a248", "a249", "a250", "a251", "a252", "a253",
    "a254", "a255", "s14", "s15", "s16", "s17", "s18", "s19", "s20", "s21", "s22", "s23", "s24", "s25", "s26", "s27", "s28", "s29", "s30", "s31", "s33", "s34", "s35", "s36",
    "s37", "s38", "s39", "s40", "s41", "s42", "s43", "s44", "s45", "s46", "s47", "s48", "s49", "s50", "s51", "s52", "s53", "s54", "s55", "s56", "s57", "s58", "s59", "s60",
    "s61", "s62", "s63", "s64", "s65", "vcc", "scc", "memory");
}

extern "C" void kernel_launch(void* const* d_in, const int* in_sizes, int n_in,
                              void* d_out, int out_size, void* d_ws, size_t ws_size,
                              hipStream_t stream) {
  conv_kernel<<<256, 256, 0, stream>>>((const char*)d_in[0], (const char*)d_in[1], (char*)d_ws, (char*)d_out);
}
```
